```python
import torch

def _softmax(x, axis=-1):
    return torch.softmax(x, dim=axis)
def _gelu(x):
    return torch.nn.functional.gelu(x, approximate="tanh")
def _silu(x):
    return torch.nn.functional.silu(x)
def _sigmoid(x):
    return torch.sigmoid(x)
def _layernorm(x, axis=-1, eps=1e-5):
    m = x.mean(dim=axis, keepdim=True)
    v = x.var(dim=axis, keepdim=True, unbiased=False)
    return (x - m) / torch.sqrt(v + eps)
def _scatter_add(tgt, idx, src, axis=0):
    return tgt.clone().index_add_(axis, idx, src)
def _segment_sum(src, seg, n):
    out = torch.zeros((n,) + src.shape[1:], dtype=src.dtype)
    return out.index_add_(0, seg, src)
def _pad(x, pw):
    flat = tuple(v for pair in reversed(pw) for v in pair)
    return torch.nn.functional.pad(x, flat)

kernel_name = 'gated_mix__spectral_fnet__glu_gated_glu__prompt'

def setup_inputs():

    _w = (lambda a: (lambda t: t.to(torch.bfloat16) if t.is_floating_point() else t)(torch.as_tensor(a)))
    def _A_setup():
        B, S, D = 1, 2048, 448
        ns = torch.arange(S)[:, None] * torch.arange(S)[None, :]
        nd = torch.arange(D)[:, None] * torch.arange(D)[None, :]
        Ws_re = torch.cos(-2.0 * torch.pi * ns / S).to(float)
        Ws_im = torch.sin(-2.0 * torch.pi * ns / S).to(float)
        Wd_re = torch.cos(-2.0 * torch.pi * nd / D).to(float)
        Wd_im = torch.sin(-2.0 * torch.pi * nd / D).to(float)
        return {
            "x": _w((lambda *s: torch.randn(*s, dtype=torch.bfloat16) * 0.05)(B, S, D)),
            "dft_seq_re": _w(Ws_re),
            "dft_seq_im": _w(Ws_im),
            "dft_hid_re": _w(Wd_re),
            "dft_hid_im": _w(Wd_im),
        }
    _A_in = _A_setup()

    def _B_setup():
        B, S, D, F = 1, 2048, 448, 1792
        return {
            "x": _w((lambda *s: torch.randn(*s, dtype=torch.bfloat16) * 0.05)(B, S, D)),
            "w_gate": _w((lambda *s: torch.randn(*s, dtype=torch.bfloat16) * 1.6)(D, F)),
            "w_up": _w((lambda *s: torch.randn(*s, dtype=torch.bfloat16) * 0.8)(D, F)),
            "w_down": _w((lambda *s: torch.randn(*s, dtype=torch.bfloat16) * 0.8)(F, D)),
        }
    _B_in = _B_setup()

    return {
        "A_x": _A_in["x"],
        "A_dft_seq_re": _A_in["dft_seq_re"],
        "A_dft_seq_im": _A_in["dft_seq_im"],
        "A_dft_hid_re": _A_in["dft_hid_re"],
        "A_dft_hid_im": _A_in["dft_hid_im"],
        "B_x": _B_in["x"],
        "B_w_gate": _B_in["w_gate"],
        "B_w_up": _B_in["w_up"],
        "B_w_down": _B_in["w_down"],
        "gate_w": _w((lambda *s: torch.randn(*s, dtype=torch.bfloat16) * 0.05)(448) * 16.0),
    }

def reference(A_x, A_dft_seq_re, A_dft_seq_im, A_dft_hid_re, A_dft_hid_im, B_x, B_w_gate, B_w_up, B_w_down, gate_w):
    def _A_ref(x, dft_seq_re, dft_seq_im, dft_hid_re, dft_hid_im):
        a = torch.einsum("bsd,ed->bse", x, dft_hid_re)
        b = torch.einsum("bsd,ed->bse", x, dft_hid_im)
        y = torch.einsum("ts,bse->bte", dft_seq_re, a) - torch.einsum("ts,bse->bte", dft_seq_im, b)
        return y

    def _B_ref(x, w_gate, w_up, w_down):
        g = torch.einsum("bsd,df->bsf", x, w_gate)
        u = torch.einsum("bsd,df->bsf", x, w_up)
        h = _sigmoid(g) * u
        return torch.einsum("bsf,fd->bsd", h, w_down)

    x = A_x
    g = _sigmoid((x * gate_w).sum(-1, keepdims=True))
    a = _A_ref(x, A_dft_seq_re, A_dft_seq_im, A_dft_hid_re, A_dft_hid_im)
    b = _B_ref(x, B_w_gate, B_w_up, B_w_down)
    return g * a + (1 - g) * b
```

```cpp
#include <hip/hip_runtime.h>
#include <hip/hip_bf16.h>
#include <stdint.h>

#define LAS __attribute__((address_space(3)))
#define GAS __attribute__((address_space(1)))

namespace fk {

typedef __bf16 bf16x2 __attribute__((ext_vector_type(2)));
typedef __bf16 bf16x4 __attribute__((ext_vector_type(4)));
typedef __bf16 bf16x8 __attribute__((ext_vector_type(8)));
typedef float f32x2 __attribute__((ext_vector_type(2)));
typedef float f32x4 __attribute__((ext_vector_type(4)));
typedef unsigned u32x2 __attribute__((ext_vector_type(2)));
typedef unsigned u32x4 __attribute__((ext_vector_type(4)));
typedef unsigned long long u64;
typedef __amdgpu_buffer_rsrc_t rsrc_t;

template <int V> struct IC { static constexpr int value = V; };
template <int B, int E, class Fn> __device__ __forceinline__ void static_for(Fn&& f) {
  if constexpr (B < E) { f(IC<B>{}); static_for<B + 1, E>(f); }
}

constexpr int NST = 37, N2 = 7, N3 = 14, N4 = 30;
constexpr int SZ1 = 22528, SZ2 = 45056, SZ3 = 30720;
constexpr int RING = 161792, GW_OFF = RING, LDS_BYTES = RING + 1024;
constexpr int st_size(int n) { return n < N2 ? SZ1 : (n < N3 ? SZ2 : SZ3); }
struct Plan { int off[NST]; int at[NST]; int last[NST]; };
constexpr Plan make_plan() {
  Plan p{};
  int cur = 0;
  for (int n = 0; n < NST; ++n) {
    if (cur + st_size(n) > RING) cur = 0;
    p.off[n] = cur;
    cur += st_size(n);
    int at = -1;
    for (int m = 0; m < n; ++m)
      if (p.off[m] < p.off[n] + st_size(n) && p.off[n] < p.off[m] + st_size(m)) at = m + 1;
    if (n > 0 && at < p.at[n - 1]) at = p.at[n - 1];
    p.at[n] = at;
  }
  for (int n = 0; n < NST; ++n) {
    int l = -1;
    for (int s = 0; s < NST; ++s) if (p.at[s] <= n - 1) l = s;
    p.last[n] = l;
  }
  return p;
}
constexpr Plan PLAN = make_plan();
template <int N> constexpr int st_off() { return PLAN.off[N]; }
template <int N> constexpr int st_at() { return PLAN.at[N]; }
constexpr int st_cnt(int cls, int n) { return n < N2 ? (cls ? 5 : 6) : (n < N3 ? 11 : (cls ? 7 : 8)); }
template <int CLS, int N> constexpr int st_wait() {
  int c = 0;
  for (int s = N + 1; s <= PLAN.last[N]; ++s) c += st_cnt(CLS, s);
  return c;
}
constexpr bool plan_ok() {
  for (int n = 0; n < NST; ++n) {
    if (PLAN.last[n] < n) return false;
    int c = 0;
    for (int s = n + 1; s <= PLAN.last[n]; ++s) c += st_cnt(0, s);
    if (c > 60) return false;
  }
  return true;
}
static_assert(plan_ok(), "stage plan");

constexpr int B_FLAG1 = 8;
constexpr int N_POLL1 = 10;
constexpr int B_FLAG2 = 18;

constexpr unsigned WS_EPOCH = 0;
constexpr unsigned WS_FLAG1 = 4096;
constexpr unsigned WS_FLAG2 = 8192;
constexpr unsigned WS_LOGIT = 16384;
constexpr unsigned WS_ABT = 131072;
constexpr unsigned WS_H = 4u << 20;
constexpr unsigned WS_INBOX = 12u << 20;
constexpr unsigned WS_END = 20u << 20;
constexpr unsigned SENT = 0xFFFFFFFFu;
constexpr int SPIN_MAX = 1 << 20;
constexpr int AUX_SC1 = 16;

#define TS(idx) do { } while (0)
#define RT(idx) do { } while (0)

#define BAR() do { asm volatile("" ::: "memory"); __builtin_amdgcn_s_barrier(); asm volatile("" ::: "memory"); } while (0)
#define WAITVM(n) asm volatile("s_waitcnt vmcnt(%0)" ::"n"(n) : "memory")
#define WAITLGKM0() do { __builtin_amdgcn_s_waitcnt(0xC07F); asm volatile("" ::: "memory"); } while (0)
#define SBAR() __builtin_amdgcn_sched_barrier(0)

__device__ __forceinline__ rsrc_t mkrs(const void* p, unsigned bytes) {
  return __builtin_amdgcn_make_buffer_rsrc((void*)p, 0, (int)bytes, 0x00020000);
}
template <int AUX> __device__ __forceinline__ void dma(rsrc_t rs, unsigned voff, unsigned soff, LAS char* l) {
  __builtin_amdgcn_raw_ptr_buffer_load_lds(rs, (LAS void*)l, 16, voff, soff, 0, AUX);
}
__device__ __forceinline__ bf16x8 ldsr(LAS char* p) { return *(LAS bf16x8*)p; }
__device__ __forceinline__ bf16x8 ldstr(LAS char* p0, LAS char* p1) {
  bf16x4 a = __builtin_amdgcn_ds_read_tr16_b64_v4bf16((LAS bf16x4*)p0);
  bf16x4 b = __builtin_amdgcn_ds_read_tr16_b64_v4bf16((LAS bf16x4*)p1);
  return __builtin_shufflevector(a, b, 0, 1, 2, 3, 4, 5, 6, 7);
}
__device__ __forceinline__ f32x4 mfma(bf16x8 a, bf16x8 b, f32x4 c) { return __builtin_amdgcn_mfma_f32_16x16x32_bf16(a, b, c, 0, 0, 0); }
__device__ __forceinline__ unsigned pk2(float x, float y) {
  f32x2 v = {x, y};
  return __builtin_bit_cast(unsigned, __builtin_convertvector(v, bf16x2));
}
__device__ __forceinline__ float bflo(unsigned d) { return __builtin_bit_cast(float, d << 16); }
__device__ __forceinline__ float bfhi(unsigned d) { return __builtin_bit_cast(float, d & 0xFFFF0000u); }
__device__ __forceinline__ u64 ld_flag(const char* ws, unsigned off) {
  return __hip_atomic_load((const GAS u64*)(ws + off), __ATOMIC_RELAXED, __HIP_MEMORY_SCOPE_AGENT);
}
__device__ __forceinline__ void st_flag(char* ws, unsigned off, u64 v) {
  __hip_atomic_store((GAS u64*)(ws + off), v, __ATOMIC_RELAXED, __HIP_MEMORY_SCOPE_AGENT);
}
__device__ __forceinline__ void wait_flag(const char* ws, unsigned off, u64 v, u64 tag) {
  for (int it = 0; it < SPIN_MAX; ++it) {
    if (__builtin_amdgcn_ballot_w64(v != tag) == 0) break;
    __builtin_amdgcn_s_sleep(2);
    v = ld_flag(ws, off);
  }
  asm volatile("" ::: "memory");
}
template <int NR, int NM> __device__ __forceinline__ void interleave() {
  static_for<0, NM>([&](auto K) {
    constexpr int k = K.value, cnt = NR / NM + (k < NR % NM ? 1 : 0);
    __builtin_amdgcn_sched_group_barrier(0x008, 1, 0);
    if constexpr (cnt > 0) __builtin_amdgcn_sched_group_barrier(0x100, cnt, 0);
  });
}

__device__ __forceinline__ int late(int x) { asm volatile("" : "+v"(x)); return x; }

struct Geo { int b, r, q, i, j, I, lane, wid; };

template <int CLS>
__device__ __forceinline__ void loader(const Geo& G, LAS char* lds, char* ws, const void* Ax, const void* Cp, const void* Wdp,
                                       const void* Wg, const void* Wu, const void* Wdn) {
  const int lane = G.lane, wid = G.wid, lw = G.wid - 4, q = G.q, j = G.j, I = G.I;
  (void)wid;
  const u64 epoch = ld_flag(ws, WS_EPOCH);
  const rsrc_t rA = mkrs(Ax, 2048u * 896u), rWd = mkrs(Wdp, 448u * 896u);
  const rsrc_t rWg = mkrs(Wg, 448u * 3584u), rWu = mkrs(Wu, 448u * 3584u), rC = mkrs(Cp, 2048u * 4096u);
  const rsrc_t rWs = mkrs(ws, WS_END), rWdn = mkrs(Wdn, 1792u * 896u);
  constexpr int NB = CLS ? 3 : 4;
  const int blk0 = CLS ? 8 + 3 * (lw - 2) : 4 * lw;
  const int r8 = lane >> 3, ch = lane & 7;
  unsigned vA[2], vWd[NB], vX[4], vW[7], vC[4], vWdn[NB];
  const int sig = 2 * I + (q & 1);
#pragma unroll
  for (int n = 0; n < 2; ++n) {
    const int row = 16 * lw + 8 * n + r8;
    const int f = ((row >> 1) & 1) | (((row >> 3) & 3) << 1);
    vA[n] = (64 * sig + row) * 896u + ((ch ^ f) << 4);
  }
#pragma unroll
  for (int n = 0; n < NB; ++n) {
    const int row = 8 * (blk0 + n) + r8;
    vWd[n] = (112 * j + row) * 896u + ((ch ^ ((row >> 1) & 7)) << 4);
    const int c16 = (blk0 + n) * 64 + lane, pr = c16 / 14, cc = c16 % 14;
    vWdn[n] = (448 * q + pr) * 896u + 224u * j + cc * 16u;
  }
#pragma unroll
  for (int n = 0; n < 4; ++n) {
    const int row = 32 * lw + 8 * n + r8;
    const unsigned sw = (ch ^ ((row >> 1) & 7)) << 4;
    vX[n] = (128 * I + row) * 896u + sw;
    vC[n] = (128 * I + row) * 4096u + sw + 2048u * (q & 1);
  }
#pragma unroll
  for (int n = 0; n < 7; ++n) {
    const int idx = 7 * lw + n, ii = idx >= 14 ? idx - 14 : idx;
    const int c16 = ii * 64 + lane, pr = c16 / 14, cc = c16 % 14;
    const int rho = (pr & ~12) | ((pr & 8) >> 1) | ((pr & 4) << 1);
    vW[n] = rho * 3584u + cc * 16u + (448u * q + 112u * j) * 2u;
  }
  const rsrc_t rW = lw < 2 ? rWg : rWu;
  const unsigned vAB = WS_ABT + (q * 4 + j) * 16u * 14336u + blk0 * 1024u + lane * 16u;
  const unsigned vH = WS_H + (I * 4 + q) * 114688u + lw * 4096u + lane * 16u;
  const unsigned fo1 = WS_FLAG1 + ((q * 4 + j) * 16 + (lane & 15)) * 8u;
  const unsigned fo2 = WS_FLAG2 + ((I * 4 + q) * 4 + (lane & 3)) * 8u;
  u64 tag = 0;

  auto issue = [&](auto S) {
    constexpr int s = S.value;
    LAS char* slot = lds + st_off<s>();
    if constexpr (s < N2) {
#pragma unroll
      for (int n = 0; n < 2; ++n) dma<0>(rA, vA[n], 128 * s, slot + (2 * lw + n) * 1024);
#pragma unroll
      for (int n = 0; n < NB; ++n) dma<0>(rWd, vWd[n], 128 * s, slot + 8192 + (blk0 + n) * 1024);
    } else if constexpr (s < N3) {
      constexpr int k = s - N2;
#pragma unroll
      for (int n = 0; n < 4; ++n) dma<0>(rA, vX[n], 128 * k, slot + (4 * lw + n) * 1024);
#pragma unroll
      for (int n = 0; n < 7; ++n) dma<0>(rW, vW[n], 229376 * k, slot + 16384 + (7 * lw + n) * 1024);
    } else if constexpr (s < N4) {
      constexpr int k = s - N3;
      if constexpr (k == 0) wait_flag(ws, fo1, ld_flag(ws, fo1), tag);
#pragma unroll
      for (int n = 0; n < 4; ++n) dma<0>(rC, vC[n], 128 * k, slot + (4 * lw + n) * 1024);
#pragma unroll
      for (int n = 0; n < NB; ++n) dma<AUX_SC1>(rWs, vAB, 14336 * k + 1024 * n, slot + 16384 + (blk0 + n) * 1024);
    } else {
      constexpr int k = s - N4;
      if constexpr (k == 0) wait_flag(ws, fo2, ld_flag(ws, fo2), tag);
#pragma unroll
      for (int n = 0; n < 4; ++n) dma<AUX_SC1>(rWs, vH, 16384 * k + 1024 * n, slot + (4 * lw + n) * 1024);
#pragma unroll
      for (int n = 0; n < NB; ++n) dma<0>(rWdn, vWdn[n], 57344 * k, slot + 16384 + (blk0 + n) * 1024);
    }
  };
  static_for<0, NST>([&](auto S) { if constexpr (st_at<S.value>() == -1) issue(S); });
  static_for<0, NST>([&](auto N) {
    constexpr int n = N.value;
    WAITVM((st_wait<CLS, n>()));
    BAR();
    if constexpr (n == 0) {
      const unsigned lo = __builtin_amdgcn_readfirstlane((unsigned)epoch), hi = __builtin_amdgcn_readfirstlane((unsigned)(epoch >> 32));
      tag = (((u64)hi << 32) | lo) * 6364136223846793005ull + 1442695040888963407ull;
    }
    TS(n);
    static_for<0, NST>([&](auto S) { if constexpr (st_at<S.value>() == n) issue(S); });
  });
  if (G.b == 0 && lw == 3) {
#pragma unroll
    for (int k = 0; k < 4; ++k) {
      const unsigned fo = WS_FLAG1 + (k * 64 + lane) * 8u;
      wait_flag(ws, fo, ld_flag(ws, fo), tag);
    }
    if (lane == 0) st_flag(ws, WS_EPOCH, tag);
  }
}

template <int EH>
__device__ __forceinline__ void g1_phase(const Geo& G, LAS char* lds, char* ws, rsrc_t rWs, u64 epoch, u64& tag) {
  const int lane = G.lane, wid = G.wid, cw = G.wid, g = lane >> 4, c = lane & 15, sh = cw >> 1;
  (void)wid;
  f32x4 acc[2][4];
#pragma unroll
  for (int a = 0; a < 2; ++a)
#pragma unroll
    for (int k = 0; k < 4; ++k) acc[a][k] = f32x4{0, 0, 0, 0};
  const int fA = ((c >> 1) & 1) | ((c >> 2) << 1);
  const int oA0 = (32 * sh + 8 * (c >> 2) + (c & 3)) * 128 + ((g ^ fA) << 4), oA1 = oA0 ^ 64;
  const int oB0 = 8192 + (64 * EH + c) * 128 + ((g ^ (c >> 1)) << 4), oB1 = oB0 ^ 64;
  const int oG = GW_OFF + 16 * g;
  bf16x8 fa[2][2], fb[2][4];
  auto rd = [&](auto N, auto H) {
    constexpr int n = N.value, h = H.value;
    LAS char* slot = lds + st_off<n>();
#pragma unroll
    for (int a = 0; a < 2; ++a) fa[h][a] = ldsr(slot + (h ? oA1 : oA0) + a * 512);
#pragma unroll
    for (int k = 0; k < 4; ++k) {
      if (EH && k == 3) fb[h][k] = ldsr(lds + oG + 128 * n + 64 * h);
      else fb[h][k] = ldsr(slot + (h ? oB1 : oB0) + k * 2048);
    }
  };
  auto mm = [&](auto H) {
    constexpr int h = H.value;
#pragma unroll
    for (int k = 0; k < 4; ++k)
#pragma unroll
      for (int a = 0; a < 2; ++a) acc[a][k] = mfma(fa[h][a], fb[h][k], acc[a][k]);
  };
  BAR();
  {
    const unsigned lo = __builtin_amdgcn_readfirstlane((unsigned)epoch), hi = __builtin_amdgcn_readfirstlane((unsigned)(epoch >> 32));
    tag = (((u64)hi << 32) | lo) * 6364136223846793005ull + 1442695040888963407ull;
  }
  TS(0);
  rd(IC<0>{}, IC<0>{});
  static_for<0, N2>([&](auto N) {
    constexpr int n = N.value;
    rd(N, IC<1>{});
    mm(IC<0>{});
    interleave<6, 8>();
    SBAR();
    WAITLGKM0();
    BAR();
    TS(n + 1);
    if constexpr (n + 1 < N2) rd(IC<n + 1>{}, IC<0>{});
    mm(IC<1>{});
    if constexpr (n + 1 < N2) interleave<6, 8>();
    SBAR();
  });
  const int p = G.q >> 1;
  const unsigned base = WS_ABT + (((2 * p + G.r) * 4 + G.j) * 16 + 2 * G.i + (G.q & 1)) * 14336u + sh * 7168u + EH * 4096u + lane * 16u;
  const float sg = p ? -1.f : 1.f;
#pragma unroll
  for (int k = 0; k < (EH ? 3 : 4); ++k) {
    const f32x4 x = acc[0][k] * sg, y = acc[1][k] * sg;
    const u32x4 v = {pk2(x[0], x[1]), pk2(x[2], x[3]), pk2(y[0], y[1]), pk2(y[2], y[3])};
    __builtin_amdgcn_raw_buffer_store_b128(v, rWs, base + k * 1024, 0, AUX_SC1);
  }
  if (EH && c == 0) {
    const unsigned lo = WS_LOGIT + G.b * 256u + (32 * sh + 8 * g) * 4u;
    __builtin_amdgcn_raw_buffer_store_b128(__builtin_bit_cast(u32x4, acc[0][3]), rWs, lo, 0, AUX_SC1);
    __builtin_amdgcn_raw_buffer_store_b128(__builtin_bit_cast(u32x4, acc[1][3]), rWs, lo + 16, 0, AUX_SC1);
  }
}

__device__ __forceinline__ void compute(const Geo& G, LAS char* lds, char* ws, const void* gw, void* out) {
  const int lane = G.lane, wid = G.wid, cw = G.wid, q = G.q, j = G.j, I = G.I, b = G.b;
  (void)wid;
  RT(62);
  TS(61);
  const u64 epoch = ld_flag(ws, WS_EPOCH);
  const rsrc_t rWs = mkrs(ws, WS_END);
#pragma unroll
  for (int d = 1; d < 4; ++d) {
    const u32x4 s4 = {SENT, SENT, SENT, SENT};
    const unsigned o = WS_INBOX + b * 32768u + cw * 2048u + lane * 16u + ((q + d) & 3) * 8192u;
    __builtin_amdgcn_raw_buffer_store_b128(s4, rWs, o, 0, AUX_SC1);
    __builtin_amdgcn_raw_buffer_store_b128(s4, rWs, o + 1024, 0, AUX_SC1);
  }
  if (cw == 1) {
    const rsrc_t rG = mkrs(gw, 896u);
    const u32x4 v = __builtin_amdgcn_raw_buffer_load_b128(rG, lane * 16u, 0, 0);
    *(LAS u32x4*)(lds + GW_OFF + lane * 16) = v;
    WAITLGKM0();
  }
  u64 tag;
  if (cw & 1) g1_phase<1>(G, lds, ws, rWs, epoch, tag);
  else g1_phase<0>(G, lds, ws, rWs, epoch, tag);

  const unsigned myflag1 = WS_FLAG1 + (((2 * (q >> 1) + G.r) * 4 + j) * 16 + 2 * G.i + (q & 1)) * 8u;
  float lg[2];
  f32x4 hg[2][7], hu[2][7];
#pragma unroll
  for (int a = 0; a < 2; ++a)
#pragma unroll
    for (int m = 0; m < 7; ++m) { hg[a][m] = f32x4{0, 0, 0, 0}; hu[a][m] = f32x4{0, 0, 0, 0}; }
  {
    const int l2 = late(lane), g = l2 >> 4, c = l2 & 15;
    const int oT0 = (32 * cw + c) * 128 + ((g ^ (c >> 1)) << 4), oT1 = oT0 ^ 64;
    const int oWb = 16384 + (16 * (g >> 1) + 4 * (g & 1) + (c >> 2)) * 224 + 8 * (c & 3);
    const int oWP = oWb + 32 * (j & 1), oWS = oWb + ((j & 1) ? 0 : 192);
    bf16x8 t[2][2], wg[3], wu[3];
    u64 fv = 0;
    auto rdW = [&](auto N, auto U) {
      constexpr int n = N.value, u = U.value, kap = u / 7, m = u % 7, bi = (14 * (n - N2) + u) % 3;
      LAS char* p = lds + st_off<n>() + (m < 6 ? oWP + 32 * m : oWS) + kap * 7168;
      wg[bi] = ldstr(p, p + 1792);
      wu[bi] = ldstr(p + 14336, p + 14336 + 1792);
    };
    auto rdT = [&](auto N, auto H) {
      constexpr int n = N.value, h = H.value;
#pragma unroll
      for (int a = 0; a < 2; ++a) t[h][a] = ldsr(lds + st_off<n>() + (h ? oT1 : oT0) + a * 2048);
    };
    rdT(IC<N2>{}, IC<0>{});
    rdW(IC<N2>{}, IC<0>{});
    rdW(IC<N2>{}, IC<1>{});
    static_for<N2, N3>([&](auto N) {
      constexpr int n = N.value;
      if constexpr (n == N_POLL1 || n == N_POLL1 + 1) {
        const int lp = late(lane), qq = lp & 3;
        const unsigned fo4 = WS_FLAG1 + (((2 * (qq >> 1) + G.r) * 4 + j) * 16 + 2 * G.i + (qq & 1)) * 8u;
        if constexpr (n == N_POLL1) fv = ld_flag(ws, fo4);
        else wait_flag(ws, fo4, fv, tag);
      }
      if constexpr (n == N_POLL1 + 1) {
        const unsigned lgo = WS_LOGIT + ((b & ~1) | (cw >> 1)) * 256u + (32 * (cw & 1) + (late(lane) & 15)) * 4u;
#pragma unroll
        for (int a = 0; a < 2; ++a) lg[a] = __builtin_bit_cast(float, __builtin_amdgcn_raw_buffer_load_b32(rWs, lgo + 64 * a, 0, AUX_SC1));
      }
      static_for<0, 14>([&](auto U) {
        constexpr int u = U.value, kap = u / 7, m = u % 7, bi = (14 * (n - N2) + u) % 3;
        if constexpr (u == 12) {
          WAITLGKM0();
          if constexpr (n + 1 == B_FLAG1) WAITVM(0);
          BAR();
          if constexpr (n + 1 == B_FLAG1) { if (cw == 0 && lane == 0) st_flag(ws, myflag1, tag); }
          TS(n + 1);
        }
        constexpr bool nxt = n + 1 < N3;
        if constexpr (u + 2 < 14) rdW(N, IC<u + 2>{});
        else if constexpr (nxt) rdW(IC<n + 1>{}, IC<u + 2 - 14>{});
        if constexpr (u == 2) rdT(N, IC<1>{});
        if constexpr (u == 12 && nxt) rdT(IC<n + 1>{}, IC<0>{});
#pragma unroll
        for (int a = 0; a < 2; ++a) {
          hg[a][m] = mfma(wg[bi], t[kap][a], hg[a][m]);
          hu[a][m] = mfma(wu[bi], t[kap][a], hu[a][m]);
        }
        constexpr int nr = ((u + 2 < 14 || nxt) ? 4 : 0) + ((u == 2 || (u == 12 && nxt)) ? 2 : 0);
        if constexpr (nr > 0) interleave<nr, 4>();
        SBAR();
      });
    });
  }
  TS(40);
  float gs[2];
  {
    const unsigned hb = WS_H + (I * 4 + q) * 114688u + (2 * cw) * 1024u + late(lane) * 16u;
    const unsigned hp = hb + ((7 * j + 1) >> 1) * 8192u;
    const unsigned hs = hb + ((j & 1) ? ((7 * j) >> 1) * 8192u + 8u : (((7 * j) >> 1) + 3) * 8192u);
#pragma unroll
    for (int a = 0; a < 2; ++a) {
      const float L2E = 1.44269504088896341f;
      gs[a] = __builtin_amdgcn_rcpf(1.f + __builtin_amdgcn_exp2f(-L2E * lg[a]));
      const float inv = fminf(1.f + __builtin_amdgcn_exp2f(L2E * lg[a]), 1e30f);
      unsigned h2[7][2];
#pragma unroll
      for (int m = 0; m < 7; ++m) {
        float h[4];
#pragma unroll
        for (int r = 0; r < 4; ++r) {
          const float e = __builtin_amdgcn_exp2f(-L2E * hg[a][m][r]);
          h[r] = hu[a][m][r] * __builtin_amdgcn_rcpf(__builtin_fmaf(e, inv, inv));
        }
        h2[m][0] = pk2(h[0], h[1]);
        h2[m][1] = pk2(h[2], h[3]);
      }
#pragma unroll
      for (int v = 0; v < 3; ++v) {
        const u32x4 d = {h2[2 * v][0], h2[2 * v][1], h2[2 * v + 1][0], h2[2 * v + 1][1]};
        __builtin_amdgcn_raw_buffer_store_b128(d, rWs, hp + v * 8192 + a * 1024, 0, AUX_SC1);
      }
      const u32x2 d2 = {h2[6][0], h2[6][1]};
      __builtin_amdgcn_raw_buffer_store_b64(d2, rWs, hs + a * 1024, 0, AUX_SC1);
    }
  }
  TS(41);

  f32x4 acc[2][7];
#pragma unroll
  for (int a = 0; a < 2; ++a)
#pragma unroll
    for (int m = 0; m < 7; ++m) acc[a][m] = f32x4{0, 0, 0, 0};
  {
    const int l3 = late(lane), g = l3 >> 4, c = l3 & 15;
    const int oT0 = (32 * cw + c) * 128 + ((g ^ (c >> 1)) << 4), oT1 = oT0 ^ 64;
    const int oE = 16384 + l3 * 16;
    const int oH = 2 * cw * 1024 + l3 * 16;
    const int oD = 16384 + (4 * g + (c >> 2)) * 224 + 8 * (c & 3);
    const unsigned myflag2 = WS_FLAG2 + ((I * 4 + q) * 4 + j) * 8u;
    bf16x8 ft[2][2], fe[2][7];
    auto rd = [&](auto N, auto H) {
      constexpr int n = N.value, h = H.value;
      LAS char* slot = lds + st_off<n>();
      if constexpr (n < N4) {
#pragma unroll
        for (int a = 0; a < 2; ++a) ft[h][a] = ldsr(slot + (h ? oT1 : oT0) + a * 2048);
#pragma unroll
        for (int m = 0; m < 7; ++m) fe[h][m] = ldsr(slot + oE + h * 7168 + m * 1024);
      } else {
#pragma unroll
        for (int a = 0; a < 2; ++a) ft[h][a] = ldsr(slot + oH + h * 8192 + a * 1024);
#pragma unroll
        for (int m = 0; m < 7; ++m) {
          LAS char* p = slot + oD + h * 7168 + m * 32;
          fe[h][m] = ldstr(p, p + 3584);
        }
      }
    };
    auto mm = [&](auto H) {
      constexpr int h = H.value;
#pragma unroll
      for (int m = 0; m < 7; ++m)
#pragma unroll
        for (int a = 0; a < 2; ++a) acc[a][m] = mfma(fe[h][m], ft[h][a], acc[a][m]);
    };
    rd(IC<N3>{}, IC<0>{});
    static_for<N3, NST>([&](auto N) {
      constexpr int n = N.value;
      rd(N, IC<1>{});
      mm(IC<0>{});
      interleave<(n < N4 ? 9 : 16), 14>();
      SBAR();
      if constexpr (n + 1 < NST) {
        WAITLGKM0();
        if constexpr (n + 1 == B_FLAG2) WAITVM(0);
        BAR();
        if constexpr (n + 1 == B_FLAG2) { if (cw == 0 && lane == 0) st_flag(ws, myflag2, tag); }
        TS(n + 1);
        rd(IC<n + 1>{}, IC<0>{});
      }
      mm(IC<1>{});
      if constexpr (n + 1 < NST) interleave<(n + 1 < N4 ? 9 : 16), 14>();
      SBAR();
      if constexpr (n + 1 == N4) {
#pragma unroll
        for (int a = 0; a < 2; ++a)
#pragma unroll
          for (int m = 0; m < 7; ++m) acc[a][m] *= gs[a];
        SBAR();
      }
    });
  }
  TS(42);

  const int l4 = late(lane), g = l4 >> 4, c = l4 & 15;
  const unsigned inb = WS_INBOX + b * 32768u + cw * 2048u + l4 * 16u;
  f32x4 own[4];
#pragma unroll
  for (int k = 0; k < 4; ++k) own[k] = f32x4{0, 0, 0, 0};
  static_for<0, 4>([&](auto Q) {
    constexpr int qd = Q.value, a = qd & 1, mb = (qd >> 1) * 4;
    if (qd != q) {
      unsigned d[8];
#pragma unroll
      for (int k = 0; k < 4; ++k) {
        if (mb + k < 7) {
          const f32x4 x = acc[a][mb + k < 7 ? mb + k : 0];
          const unsigned u0 = pk2(x[0], x[1]), u1 = pk2(x[2], x[3]);
          d[2 * k] = u0 < SENT ? u0 : SENT - 1;
          d[2 * k + 1] = u1 < SENT ? u1 : SENT - 1;
        } else { d[2 * k] = 0; d[2 * k + 1] = 0; }
      }
      const unsigned o = WS_INBOX + ((b & ~3) | qd) * 32768u + q * 8192u + cw * 2048u + l4 * 16u;
      __builtin_amdgcn_raw_buffer_store_b128(u32x4{d[0], d[1], d[2], d[3]}, rWs, o, 0, AUX_SC1);
      __builtin_amdgcn_raw_buffer_store_b128(u32x4{d[4], d[5], d[6], d[7]}, rWs, o + 1024, 0, AUX_SC1);
    } else {
#pragma unroll
      for (int k = 0; k < 4; ++k) if (mb + k < 7) own[k] = acc[a][mb + k < 7 ? mb + k : 0];
    }
  });
  TS(43);
  u32x4 in[3][2];
  for (int it = 0; it < SPIN_MAX; ++it) {
    bool miss = false;
#pragma unroll
    for (int d = 0; d < 3; ++d) {
      const unsigned o = inb + ((q + d + 1) & 3) * 8192u;
      in[d][0] = __builtin_amdgcn_raw_buffer_load_b128(rWs, o, 0, AUX_SC1);
      in[d][1] = __builtin_amdgcn_raw_buffer_load_b128(rWs, o + 1024, 0, AUX_SC1);
    }
#pragma unroll
    for (int d = 0; d < 3; ++d)
#pragma unroll
      for (int h = 0; h < 2; ++h)
#pragma unroll
        for (int k = 0; k < 4; ++k) miss |= in[d][h][k] == SENT;
    if (__builtin_amdgcn_ballot_w64(miss) == 0) break;
    asm volatile("" ::: "memory");
  }
  TS(44);
  {
    const rsrc_t rO = mkrs(out, 2048u * 896u);
    const int mb = (q >> 1) * 4;
    const unsigned ob = (128 * I + 32 * cw + 16 * (q & 1) + c) * 896u + (112 * j + 16 * mb + 4 * g) * 2u;
#pragma unroll
    for (int k = 0; k < 4; ++k) {
      f32x4 s = own[k];
#pragma unroll
      for (int d = 0; d < 3; ++d) {
        const unsigned u0 = in[d][k >> 1][2 * (k & 1)], u1 = in[d][k >> 1][2 * (k & 1) + 1];
        s += f32x4{bflo(u0), bfhi(u0), bflo(u1), bfhi(u1)};
      }
      if (k < 3 || mb == 0) {
        const u32x2 o2 = {pk2(s[0], s[1]), pk2(s[2], s[3])};
        __builtin_amdgcn_raw_buffer_store_b64(o2, rO, ob + 32 * k, 0, 0);
      }
    }
  }
  TS(45);
  RT(63);
}

__global__ __launch_bounds__(512, 2) void fused_kernel(const void* __restrict__ Ax, const void* __restrict__ Cre, const void* __restrict__ Cim,
                                                       const void* __restrict__ Wdre, const void* __restrict__ Wdim,
                                                       const void* __restrict__ Wg, const void* __restrict__ Wu,
                                                       const void* __restrict__ Wdn, const void* __restrict__ gw, void* out, char* ws) {
  __shared__ __attribute__((aligned(1024))) char shm[LDS_BYTES];
  LAS char* lds = (LAS char*)shm;
  Geo G;
  G.lane = threadIdx.x & 63;
  G.wid = __builtin_amdgcn_readfirstlane(threadIdx.x >> 6);
  G.b = blockIdx.x;
  G.q = G.b & 3;
  G.r = (G.b >> 2) & 1;
  G.j = (G.b >> 3) & 3;
  G.i = G.b >> 5;
  G.I = 8 * G.r + G.i;
  if (G.wid >= 4) {
    const bool im = G.q >= 2;
    const void* Cp = im ? Cim : Cre;
    const void* Wdp = im ? Wdim : Wdre;
    if (G.wid < 6) loader<0>(G, lds, ws, Ax, Cp, Wdp, Wg, Wu, Wdn);
    else loader<1>(G, lds, ws, Ax, Cp, Wdp, Wg, Wu, Wdn);
  } else {
    compute(G, lds, ws, gw, out);
  }
}

}

extern "C" void kernel_launch(void* const* d_in, const int* in_sizes, int n_in, void* d_out, int out_size, void* d_ws,
                              size_t ws_size, hipStream_t stream) {
  (void)in_sizes; (void)n_in; (void)out_size; (void)ws_size;
  fk::fused_kernel<<<256, 512, 0, stream>>>(d_in[0], d_in[1], d_in[2], d_in[3], d_in[4], d_in[6], d_in[7], d_in[8], d_in[9],
                                            d_out, (char*)d_ws);
}
```

```cpp
#include <hip/hip_runtime.h>
#include <hip/hip_bf16.h>
#include <stdint.h>

#define LAS __attribute__((address_space(3)))
#define GAS __attribute__((address_space(1)))

namespace fk {

typedef __bf16 bf16x2 __attribute__((ext_vector_type(2)));
typedef __bf16 bf16x4 __attribute__((ext_vector_type(4)));
typedef __bf16 bf16x8 __attribute__((ext_vector_type(8)));
typedef float f32x2 __attribute__((ext_vector_type(2)));
typedef float f32x4 __attribute__((ext_vector_type(4)));
typedef unsigned u32x2 __attribute__((ext_vector_type(2)));
typedef unsigned u32x4 __attribute__((ext_vector_type(4)));
typedef unsigned long long u64;
typedef __amdgpu_buffer_rsrc_t rsrc_t;

template <int V> struct IC { static constexpr int value = V; };
template <int B, int E, class Fn> __device__ __forceinline__ void static_for(Fn&& f) {
  if constexpr (B < E) { f(IC<B>{}); static_for<B + 1, E>(f); }
}

constexpr int NST = 37, N2 = 7, N3 = 14, N4 = 30;
constexpr int SZ1 = 22528, SZ2 = 45056, SZ3 = 30720;
constexpr int RING = 161792, GW_OFF = RING, PF_OFF = RING + 1024, LDS_BYTES = RING + 2048;
constexpr int st_size(int n) { return n < N2 ? SZ1 : (n < N3 ? SZ2 : SZ3); }
constexpr int st_szA(int n) { return n < N2 ? SZ1 : 16384; }
constexpr int cntA(int cls, int n) { return n < N2 ? (cls ? 5 : 6) : 4; }
constexpr int cntB(int cls, int n) { return n < N2 ? 0 : (n < N3 ? 7 : (cls ? 3 : 4)); }
constexpr int N_GAP = 19, T_GAP = 17;
struct Plan { int off[NST]; int atA[NST]; int atB[NST]; };
constexpr Plan make_plan() {
  Plan p{};
  constexpr int ova[10] = {-1, -1, 0, 0, 1, 2, 3, 4, 5, 7};
  constexpr int ovb[3] = {5, 6, 8};
  int cur = 0;
  for (int n = 0; n < NST; ++n) {
    if (cur + st_size(n) > RING) cur = 0;
    p.off[n] = cur;
    cur += st_size(n);
    int a = -1, b = -1;
    const int lo = p.off[n], mid = lo + st_szA(n), hi = lo + st_size(n);
    for (int m = 0; m < n; ++m) {
      const int ml = p.off[m], mh = ml + st_size(m);
      if (ml < mid && lo < mh) a = m + 1;
      if (mid < hi && ml < hi && mid < mh) b = m + 1;
    }
    if (n < 10 && a < ova[n]) a = ova[n];
    if (n >= N2 && n < 10 && b < ovb[n - N2]) b = ovb[n - N2];
    if (n >= 10) { if (a < b) a = b; b = a; }
    if (n > 0 && a < p.atA[n - 1]) a = p.atA[n - 1];
    if (n > N2 && b < p.atB[n - 1]) b = p.atB[n - 1];
    if (n == N_GAP && a < T_GAP) a = T_GAP;
    if ((n > N_GAP || (n > N3 && n < N3 + 3)) && a <= p.atA[n - 1]) a = p.atA[n - 1] + 1;
    if (n >= 10 && b < a) b = a;
    if (n >= 10 && a < b) a = b;
    p.atA[n] = a;
    p.atB[n] = n < N2 ? a : b;
  }
  return p;
}
constexpr Plan PLAN = make_plan();
template <int N> constexpr int st_off() { return PLAN.off[N]; }
template <int N> constexpr int st_atA() { return PLAN.atA[N]; }
template <int N> constexpr int st_atB() { return PLAN.atB[N]; }
constexpr int T_EPOCH = -1;
constexpr int T_PF1 = PLAN.atA[N3] - 2, T_PF2 = PLAN.atA[N4] - 2;
constexpr int T_C0 = 7, T_C1 = 9, T_C2 = 11, T_C3 = 13;
constexpr int T_D = 20;
constexpr int pf_cnt(int a) {
  return (a == T_C0) + (a == T_C1) + (a == T_C2) + (a == T_C3) + (a == T_C0 + 1) + (a == T_C1 + 1) + (a == T_C2 + 1) + (a == T_C3 + 1) +
         (a == T_D) + (a == T_EPOCH) + (a == T_PF1) + (a == T_PF2);
}
constexpr int st_wait_c(int cls, int n) {
  int c = 0;
  bool seenA = false, seenB = false;
  for (int t = -1; t <= n - 1; ++t) {
    for (int s = 0; s < NST; ++s) {
      if (PLAN.atA[s] == t) { if (s == n) { seenA = true; c = 0; } else c += cntA(cls, s); }
      if (PLAN.atB[s] == t && cntB(cls, s) > 0) { if (s == n) { seenB = true; c = 0; } else c += cntB(cls, s); }
    }
    c += pf_cnt(t);
  }
  return seenA && (seenB || cntB(cls, n) == 0) ? c : -1;
}
template <int CLS, int N> constexpr int st_wait() { return st_wait_c(CLS, N); }
constexpr bool plan_ok() {
  for (int n = 0; n < NST; ++n)
    for (int cls = 0; cls < 2; ++cls)
      if (st_wait_c(cls, n) < 0 || st_wait_c(cls, n) > 62) return false;
  return true;
}
static_assert(plan_ok(), "stage plan");

constexpr int B_TAG = 4;
constexpr int B_FLAG1 = 9;
constexpr int N_POLL1 = 11;
constexpr int B_FLAG2 = 21;

constexpr unsigned WS_EPOCH = 0;
constexpr unsigned WS_FLAG1 = 4096;
constexpr unsigned WS_FLAG2 = 8192;
constexpr unsigned WS_LOGIT = 16384;
constexpr unsigned WS_ABT = 131072;
constexpr unsigned WS_H = 4u << 20;
constexpr unsigned WS_INBOX = 12u << 20;
constexpr unsigned WS_END = 20u << 20;
constexpr unsigned SENT = 0xFFFFFFFFu;
constexpr int SPIN_MAX = 1 << 20;
constexpr int AUX_SC1 = 16;

#define BAR() do { asm volatile("" ::: "memory"); __builtin_amdgcn_s_barrier(); asm volatile("" ::: "memory"); } while (0)
#define WAITVM(n) asm volatile("s_waitcnt vmcnt(%0)" ::"n"(n) : "memory")
#define WAITLGKM0() do { __builtin_amdgcn_s_waitcnt(0xC07F); asm volatile("" ::: "memory"); } while (0)
#define SBAR() __builtin_amdgcn_sched_barrier(0)

__device__ __forceinline__ rsrc_t mkrs(const void* p, unsigned bytes) {
  return __builtin_amdgcn_make_buffer_rsrc((void*)p, 0, (int)bytes, 0x00020000);
}
template <int AUX> __device__ __forceinline__ void dma(rsrc_t rs, unsigned voff, unsigned soff, LAS char* l) {
  __builtin_amdgcn_raw_ptr_buffer_load_lds(rs, (LAS void*)l, 16, voff, soff, 0, AUX);
}
__device__ __forceinline__ bf16x8 ldsr(LAS char* p) { return *(LAS bf16x8*)p; }
__device__ __forceinline__ bf16x8 ldstr(LAS char* p0, LAS char* p1) {
  bf16x4 a = __builtin_amdgcn_ds_read_tr16_b64_v4bf16((LAS bf16x4*)p0);
  bf16x4 b = __builtin_amdgcn_ds_read_tr16_b64_v4bf16((LAS bf16x4*)p1);
  return __builtin_shufflevector(a, b, 0, 1, 2, 3, 4, 5, 6, 7);
}
__device__ __forceinline__ f32x4 mfma(bf16x8 a, bf16x8 b, f32x4 c) { return __builtin_amdgcn_mfma_f32_16x16x32_bf16(a, b, c, 0, 0, 0); }
__device__ __forceinline__ unsigned pk2(float x, float y) {
  f32x2 v = {x, y};
  return __builtin_bit_cast(unsigned, __builtin_convertvector(v, bf16x2));
}
__device__ __forceinline__ float bflo(unsigned d) { return __builtin_bit_cast(float, d << 16); }
__device__ __forceinline__ float bfhi(unsigned d) { return __builtin_bit_cast(float, d & 0xFFFF0000u); }
__device__ __forceinline__ u64 ld_flag(const char* ws, unsigned off) {
  return __hip_atomic_load((const GAS u64*)(ws + off), __ATOMIC_RELAXED, __HIP_MEMORY_SCOPE_AGENT);
}
__device__ __forceinline__ void st_flag(char* ws, unsigned off, u64 v) {
  __hip_atomic_store((GAS u64*)(ws + off), v, __ATOMIC_RELAXED, __HIP_MEMORY_SCOPE_AGENT);
}
__device__ __forceinline__ u64 make_tag(u64 epoch) {
  const unsigned lo = __builtin_amdgcn_readfirstlane((unsigned)epoch), hi = __builtin_amdgcn_readfirstlane((unsigned)(epoch >> 32));
  return (((u64)hi << 32) | lo) * 6364136223846793005ull + 1442695040888963407ull;
}
__device__ __forceinline__ void wait_flag(const char* ws, unsigned off, u64 v, u64 tag) {
  asm volatile("" : "+v"(v));
  if (__builtin_amdgcn_ballot_w64(v != tag) != 0) {
    for (int it = 0; it < SPIN_MAX; ++it) {
      __builtin_amdgcn_s_sleep(2);
      if (__builtin_amdgcn_ballot_w64(ld_flag(ws, off) != tag) == 0) break;
    }
  }
  asm volatile("" ::: "memory");
}
template <int NR, int NM> __device__ __forceinline__ void interleave() {
  static_for<0, NM>([&](auto K) {
    constexpr int k = K.value, cnt = NR / NM + (k < NR % NM ? 1 : 0);
    __builtin_amdgcn_sched_group_barrier(0x008, 1, 0);
    if constexpr (cnt > 0) __builtin_amdgcn_sched_group_barrier(0x100, cnt, 0);
  });
}

__device__ __forceinline__ int late(int x) { asm volatile("" : "+v"(x)); return x; }

struct Geo { int b, r, q, i, j, I, lane, wid, k7, k16; };
template <int NK> __device__ __forceinline__ unsigned kstep(const Geo& G, int k) {
  const unsigned x = (NK == 7 ? G.k7 : G.k16) + k;
  return x >= NK ? x - NK : x;
}

template <int CLS>
__device__ __forceinline__ void loader(const Geo& G, LAS char* lds, char* ws, const void* Ax, const void* Cp, const void* Wdp,
                                       const void* Wg, const void* Wu, const void* Wdn) {
  const int lane = G.lane, lw = G.wid - 4, q = G.q, j = G.j, I = G.I;
  asm volatile(".Lfk_loader%0:" ::"n"(CLS));
  SBAR();
  const rsrc_t rA = mkrs(Ax, 2048u * 896u), rWd = mkrs(Wdp, 448u * 896u);
  constexpr int NB = CLS ? 3 : 4;
  const int blk0 = CLS ? 8 + 3 * (lw - 2) : 4 * lw;
  unsigned vA[2], vWd[NB], vX[4], vW[7], vC[4], vWdn[NB];
  {
    const int r8 = lane >> 3, ch = lane & 7;
    const int sig = 2 * I + (q & 1);
#pragma unroll
    for (int n = 0; n < 2; ++n) {
      const int row = 16 * lw + 8 * n + r8;
      const int f = ((row >> 1) & 1) | (((row >> 3) & 3) << 1);
      vA[n] = (64 * sig + row) * 896u + ((ch ^ f) << 4);
    }
#pragma unroll
    for (int n = 0; n < NB; ++n) {
      const int row = 8 * (blk0 + n) + r8;
      vWd[n] = (112 * j + row) * 896u + ((ch ^ ((row >> 1) & 7)) << 4);
    }
  }
  const rsrc_t rC = mkrs(Cp, 2048u * 4096u), rWs = mkrs(ws, WS_END), rWdn = mkrs(Wdn, 1792u * 896u);
  const rsrc_t rW = mkrs(lw < 2 ? Wg : Wu, 448u * 3584u);
  unsigned vAB, vH, fo1, fo2, tD, tC, tK;
  u64 tag = 0, f1 = 0, f2 = 0;

  auto issueA = [&](auto S) {
    constexpr int s = S.value;
    LAS char* slot = lds + st_off<s>();
    if constexpr (s < N2) {
#pragma unroll
      for (int n = 0; n < 2; ++n) dma<0>(rA, vA[n], 128 * kstep<7>(G, s), slot + (2 * lw + n) * 1024);
#pragma unroll
      for (int n = 0; n < NB; ++n) dma<0>(rWd, vWd[n], 128 * kstep<7>(G, s), slot + 8192 + (blk0 + n) * 1024);
    } else if constexpr (s < N3) {
      const unsigned k = kstep<7>(G, s - N2);
#pragma unroll
      for (int n = 0; n < 4; ++n) dma<0>(rA, vX[n], 128 * k, slot + (4 * lw + n) * 1024);
    } else if constexpr (s < N4) {
      const unsigned k = kstep<16>(G, s - N3);
      if constexpr (s == N3) wait_flag(ws, fo1, f1, tag);
#pragma unroll
      for (int n = 0; n < 4; ++n) dma<0>(rC, vC[n], 128 * k, slot + (4 * lw + n) * 1024);
    } else {
      const unsigned k = kstep<7>(G, s - N4);
      if constexpr (s == N4) wait_flag(ws, fo2, f2, tag);
#pragma unroll
      for (int n = 0; n < 4; ++n) dma<AUX_SC1>(rWs, vH, 16384 * k + 1024 * n, slot + (4 * lw + n) * 1024);
    }
  };
  auto issueB = [&](auto S) {
    constexpr int s = S.value;
    LAS char* slot = lds + st_off<s>();
    if constexpr (s >= N2 && s < N3) {
      const unsigned k = kstep<7>(G, s - N2);
#pragma unroll
      for (int n = 0; n < 7; ++n) dma<0>(rW, vW[n], 229376 * k, slot + 16384 + (7 * lw + n) * 1024);
    } else if constexpr (s >= N3 && s < N4) {
      const unsigned k = kstep<16>(G, s - N3);
#pragma unroll
      for (int n = 0; n < NB; ++n) dma<AUX_SC1>(rWs, vAB, 14336 * k + 1024 * n, slot + 16384 + (blk0 + n) * 1024);
    } else if constexpr (s >= N4) {
      const unsigned k = kstep<7>(G, s - N4);
#pragma unroll
      for (int n = 0; n < NB; ++n) dma<0>(rWdn, vWdn[n], 57344 * k, slot + 16384 + (blk0 + n) * 1024);
    }
  };
  auto issue_at = [&](auto T) {
    static_for<0, NST>([&](auto S) {
      if constexpr (st_atA<S.value>() == T.value) issueA(S);
      if constexpr (st_atB<S.value>() == T.value) issueB(S);
    });
  };
  auto touch = [&](rsrc_t rs, unsigned voff, unsigned soff) {
    __builtin_amdgcn_raw_ptr_buffer_load_lds(rs, (LAS void*)(lds + PF_OFF), 4, voff, soff, 0, 0);
  };
  auto vpf = [&](auto A) {
    constexpr int a = A.value;
    auto touchC = [&](int e, int par) {
      const unsigned o = tC + (((tK + 4 * e) & 15) << 7);
      touch(rC, (G.i & 1) == par ? o : 0x80000000u, 0);
    };
    if constexpr (a == T_C0) touchC(0, 0);
    if constexpr (a == T_C1) touchC(1, 0);
    if constexpr (a == T_C2) touchC(2, 0);
    if constexpr (a == T_C3) touchC(3, 0);
    if constexpr (a == T_C0 + 1) touchC(0, 1);
    if constexpr (a == T_C1 + 1) touchC(1, 1);
    if constexpr (a == T_C2 + 1) touchC(2, 1);
    if constexpr (a == T_C3 + 1) touchC(3, 1);
    if constexpr (a == T_D) touch(rWdn, tD, 0);
    if constexpr (a == T_PF1) f1 = ld_flag(ws, fo1);
    if constexpr (a == T_PF2) f2 = ld_flag(ws, fo2);
  };
  issue_at(IC<-1>{});
  const int ln = late(lane);
  u64 epoch = ld_flag(ws, WS_EPOCH);
  {
    const int r8 = ln >> 3, ch = ln & 7;
#pragma unroll
    for (int n = 0; n < 4; ++n) {
      const int row = 32 * lw + 8 * n + r8;
      const unsigned sw = (ch ^ ((row >> 1) & 7)) << 4;
      vX[n] = (128 * I + row) * 896u + sw;
      vC[n] = (128 * I + row) * 4096u + sw + 2048u * (q & 1);
    }
#pragma unroll
    for (int n = 0; n < 7; ++n) {
      const int idx = 7 * lw + n, ii = idx >= 14 ? idx - 14 : idx;
      const unsigned c16 = ii * 64 + ln, pr = (c16 * 4682u) >> 16, cc = c16 - 14u * pr;
      const unsigned rho = (pr & ~12u) | ((pr & 8) >> 1) | ((pr & 4) << 1);
      vW[n] = rho * 3584u + cc * 16u + (448u * q + 112u * j) * 2u;
    }
#pragma unroll
    for (int n = 0; n < NB; ++n) {
      const unsigned c16 = (blk0 + n) * 64 + ln, pr = (c16 * 4682u) >> 16, cc = c16 - 14u * pr;
      vWdn[n] = (448 * q + pr) * 896u + 224u * j + cc * 16u;
    }
    vAB = WS_ABT + (q * 4 + j) * 16u * 14336u + blk0 * 1024u + ln * 16u;
    vH = WS_H + (I * 4 + q) * 114688u + lw * 4096u + ln * 16u;
    fo1 = WS_FLAG1 + ((q * 4 + j) * 16 + (ln & 15)) * 8u;
    fo2 = WS_FLAG2 + ((I * 4 + q) * 4 + (ln & 3)) * 8u;
    const unsigned pid = ((G.i * 4 + j) * 4 + lw) * 49 + ln;
    tD = ln < 49 ? 448u * 896u * q + pid * 64u : 0x80000000u;
    tC = (128 * I + 32 * j + 8 * lw + ((ln >> 1) & 7)) * 4096u + 2048u * (q & 1) + (ln & 1) * 64u;
    tK = kstep<16>(G, 0) + (ln >> 4);
  }
  vpf(IC<-1>{});
  static_for<0, NST>([&](auto N) {
    constexpr int n = N.value;
    WAITVM((st_wait<CLS, n>()));
    BAR();
    if constexpr (n == B_TAG) {
      asm volatile("" : "+v"(epoch));
      tag = make_tag(epoch);
    }
    issue_at(N);
    vpf(N);
  });
  if (G.b == 0 && lw == 3) {
#pragma unroll
    for (int k = 0; k < 4; ++k) {
      const unsigned fo = WS_FLAG1 + (k * 64 + lane) * 8u;
      wait_flag(ws, fo, ld_flag(ws, fo), tag);
    }
    if (lane == 0) st_flag(ws, WS_EPOCH, tag);
  }
}

__device__ __forceinline__ void clear_inbox(const Geo& G, rsrc_t rWs) {
#pragma unroll
  for (int d = 1; d < 4; ++d) {
    const u32x4 s4 = {SENT, SENT, SENT, SENT};
    const unsigned o = WS_INBOX + G.b * 32768u + G.wid * 2048u + late(G.lane) * 16u + ((G.q + d) & 3) * 8192u;
    __builtin_amdgcn_raw_buffer_store_b128(s4, rWs, o, 0, AUX_SC1);
    __builtin_amdgcn_raw_buffer_store_b128(s4, rWs, o + 1024, 0, AUX_SC1);
  }
}
template <int EH>
__device__ __forceinline__ void g1_phase(const Geo& G, LAS char* lds, char* ws, rsrc_t rWs) {
  const int lane = G.lane, cw = G.wid, g = lane >> 4, c = lane & 15, sh = cw >> 1;
  f32x4 acc[2][4];
#pragma unroll
  for (int a = 0; a < 2; ++a)
#pragma unroll
    for (int k = 0; k < 4; ++k) acc[a][k] = f32x4{0, 0, 0, 0};
  const int fA = ((c >> 1) & 1) | ((c >> 2) << 1);
  const int oA0 = (32 * sh + 8 * (c >> 2) + (c & 3)) * 128 + ((g ^ fA) << 4), oA1 = oA0 ^ 64;
  const int oB0 = 8192 + (64 * EH + c) * 128 + ((g ^ (c >> 1)) << 4), oB1 = oB0 ^ 64;
  const int oG = GW_OFF + 16 * g;
  bf16x8 fa[2][2], fb[2][4];
  auto rd = [&](auto N, auto H) {
    constexpr int n = N.value, h = H.value;
    LAS char* slot = lds + st_off<n>();
#pragma unroll
    for (int a = 0; a < 2; ++a) fa[h][a] = ldsr(slot + (h ? oA1 : oA0) + a * 512);
#pragma unroll
    for (int k = 0; k < 4; ++k) {
      if (EH && k == 3) fb[h][k] = ldsr(lds + (oG + 128 * kstep<7>(G, n)) + 64 * h);
      else fb[h][k] = ldsr(slot + (h ? oB1 : oB0) + k * 2048);
    }
  };
  auto mm = [&](auto H) {
    constexpr int h = H.value;
#pragma unroll
    for (int k = 0; k < 4; ++k)
#pragma unroll
      for (int a = 0; a < 2; ++a) acc[a][k] = mfma(fa[h][a], fb[h][k], acc[a][k]);
  };
  BAR();
  rd(IC<0>{}, IC<0>{});
  static_for<0, N2>([&](auto N) {
    constexpr int n = N.value;
    rd(N, IC<1>{});
    mm(IC<0>{});
    interleave<6, 8>();
    SBAR();
    WAITLGKM0();
    BAR();
    if constexpr (n == 0) clear_inbox(G, rWs);
    if constexpr (n + 1 < N2) rd(IC<n + 1>{}, IC<0>{});
    mm(IC<1>{});
    if constexpr (n + 1 < N2) interleave<6, 8>();
    SBAR();
  });
  const int p = G.q >> 1;
  const unsigned base = WS_ABT + (((2 * p + G.r) * 4 + G.j) * 16 + 2 * G.i + (G.q & 1)) * 14336u + sh * 7168u + EH * 4096u + lane * 16u;
  const float sg = p ? -1.f : 1.f;
#pragma unroll
  for (int k = 0; k < (EH ? 3 : 4); ++k) {
    const f32x4 x = acc[0][k] * sg, y = acc[1][k] * sg;
    const u32x4 v = {pk2(x[0], x[1]), pk2(x[2], x[3]), pk2(y[0], y[1]), pk2(y[2], y[3])};
    __builtin_amdgcn_raw_buffer_store_b128(v, rWs, base + k * 1024, 0, AUX_SC1);
  }
  if (EH && c == 0) {
    const unsigned lo = WS_LOGIT + G.b * 256u + (32 * sh + 8 * g) * 4u;
    __builtin_amdgcn_raw_buffer_store_b128(__builtin_bit_cast(u32x4, acc[0][3]), rWs, lo, 0, AUX_SC1);
    __builtin_amdgcn_raw_buffer_store_b128(__builtin_bit_cast(u32x4, acc[1][3]), rWs, lo + 16, 0, AUX_SC1);
  }
}

__device__ __forceinline__ void compute(const Geo& G, LAS char* lds, char* ws, const void* gw, void* out, const void* Wg,
                                        const void* Wu, const void* Wdn, const void* Cp) {
  const int lane = G.lane, cw = G.wid, q = G.q, j = G.j, I = G.I, b = G.b;
  const u64 epoch = ld_flag(ws, WS_EPOCH);
  const rsrc_t rWs = mkrs(ws, WS_END);
  {
    u64 pc;
    unsigned len;
    asm volatile("s_getpc_b64 %0\n.Lfk_code_begin:\n\ts_mov_b32 %1, .Lfk_code_end-.Lfk_code_begin" : "=s"(pc), "=s"(len));
    const unsigned o = lane < 7 ? (((b >> 3) * 4 + cw) * 7 + lane) * 64u : 0x80000000u;
    __builtin_amdgcn_raw_ptr_buffer_load_lds(mkrs((const void*)pc, len), (LAS void*)(lds + PF_OFF), 4, o, 0, 0, 0);
  }
  if (cw == 2) {
    const unsigned last = lane & 1;
    const char* pa = (const char*)Wg + last * (448u * 3584u - 4);
    if ((lane >> 1) == 1) pa = (const char*)Wu + last * (448u * 3584u - 4);
    if ((lane >> 1) == 2) pa = (const char*)Wdn + 401408u * q + last * 401404u;
    if ((lane >> 1) == 3) pa = (const char*)Cp + 524288u * I + last * 524284u;
    if (lane < 8) __builtin_amdgcn_global_load_lds((const GAS void*)pa, (LAS void*)(lds + PF_OFF + 512), 4, 0, 0);
  }
  if (cw == 1) {
    const rsrc_t rG = mkrs(gw, 896u);
    const u32x4 v = __builtin_amdgcn_raw_buffer_load_b128(rG, lane * 16u, 0, 0);
    *(LAS u32x4*)(lds + GW_OFF + lane * 16) = v;
    WAITLGKM0();
  }
  __builtin_amdgcn_s_waitcnt(0x0F70);
  const u64 tag = make_tag(epoch);
  if (cw & 1) g1_phase<1>(G, lds, ws, rWs);
  else g1_phase<0>(G, lds, ws, rWs);

  const unsigned myflag1 = WS_FLAG1 + (((2 * (q >> 1) + G.r) * 4 + j) * 16 + 2 * G.i + (q & 1)) * 8u;
  float lg[2];
  f32x4 hg[2][7], hu[2][7];
#pragma unroll
  for (int a = 0; a < 2; ++a)
#pragma unroll
    for (int m = 0; m < 7; ++m) { hg[a][m] = f32x4{0, 0, 0, 0}; hu[a][m] = f32x4{0, 0, 0, 0}; }
  {
    const int l2 = late(lane), g = l2 >> 4, c = l2 & 15;
    const int oT0 = (32 * cw + c) * 128 + ((g ^ (c >> 1)) << 4), oT1 = oT0 ^ 64;
    const int oWb = 16384 + (16 * (g >> 1) + 4 * (g & 1) + (c >> 2)) * 224 + 8 * (c & 3);
    const int oWP = oWb + 32 * (j & 1), oWS = oWb + ((j & 1) ? 0 : 192);
    bf16x8 t[2][2], wg[3], wu[3];
    u64 fv = 0;
    auto rdW = [&](auto N, auto U) {
      constexpr int n = N.value, u = U.value, kap = u / 7, m = u % 7, bi = (14 * (n - N2) + u) % 3;
      LAS char* p = lds + st_off<n>() + (m < 6 ? oWP + 32 * m : oWS) + kap * 7168;
      wg[bi] = ldstr(p, p + 1792);
      wu[bi] = ldstr(p + 14336, p + 14336 + 1792);
    };
    auto rdT = [&](auto N, auto H) {
      constexpr int n = N.value, h = H.value;
#pragma unroll
      for (int a = 0; a < 2; ++a) t[h][a] = ldsr(lds + st_off<n>() + (h ? oT1 : oT0) + a * 2048);
    };
    rdT(IC<N2>{}, IC<0>{});
    rdW(IC<N2>{}, IC<0>{});
    rdW(IC<N2>{}, IC<1>{});
    SBAR();
    static_for<N2, N3>([&](auto N) {
      constexpr int n = N.value;
      if constexpr (n == N_POLL1 || n == N_POLL1 + 1) {
        const int lp = late(lane), qq = lp & 3;
        const unsigned fo4 = WS_FLAG1 + (((2 * (qq >> 1) + G.r) * 4 + j) * 16 + 2 * G.i + (qq & 1)) * 8u;
        if constexpr (n == N_POLL1) fv = ld_flag(ws, fo4);
        else wait_flag(ws, fo4, fv, tag);
      }
      if constexpr (n == N_POLL1 + 1) {
        const unsigned lgo = WS_LOGIT + ((b & ~1) | (cw >> 1)) * 256u + (32 * (cw & 1) + (late(lane) & 15)) * 4u;
#pragma unroll
        for (int a = 0; a < 2; ++a) lg[a] = __builtin_bit_cast(float, __builtin_amdgcn_raw_buffer_load_b32(rWs, lgo + 64 * a, 0, AUX_SC1));
      }
      static_for<0, 14>([&](auto U) {
        constexpr int u = U.value, kap = u / 7, m = u % 7, bi = (14 * (n - N2) + u) % 3;
        if constexpr (u == 12) {
          WAITLGKM0();
          if constexpr (n + 1 == B_FLAG1) WAITVM(0);
          BAR();
          if constexpr (n + 1 == B_FLAG1) { if (cw == 0 && lane == 0) st_flag(ws, myflag1, tag); }
        }
        constexpr bool nxt = n + 1 < N3;
        if constexpr (u + 2 < 14) rdW(N, IC<u + 2>{});
        else if constexpr (nxt) rdW(IC<n + 1>{}, IC<u + 2 - 14>{});
        if constexpr (u == 2) rdT(N, IC<1>{});
        if constexpr (u == 12 && nxt) rdT(IC<n + 1>{}, IC<0>{});
#pragma unroll
        for (int a = 0; a < 2; ++a) {
          hg[a][m] = mfma(wg[bi], t[kap][a], hg[a][m]);
          hu[a][m] = mfma(wu[bi], t[kap][a], hu[a][m]);
        }
        constexpr int nr = ((u + 2 < 14 || nxt) ? 4 : 0) + ((u == 2 || (u == 12 && nxt)) ? 2 : 0);
        if constexpr (nr > 0) interleave<nr, 4>();
        SBAR();
      });
    });
  }
  float gs[2];
  {
    const unsigned hb = WS_H + (I * 4 + q) * 114688u + (2 * cw) * 1024u + late(lane) * 16u;
    const unsigned hp = hb + ((7 * j + 1) >> 1) * 8192u;
    const unsigned hs = hb + ((j & 1) ? ((7 * j) >> 1) * 8192u + 8u : (((7 * j) >> 1) + 3) * 8192u);
#pragma unroll
    for (int a = 0; a < 2; ++a) {
      const float L2E = 1.44269504088896341f;
      gs[a] = __builtin_amdgcn_rcpf(1.f + __builtin_amdgcn_exp2f(-L2E * lg[a]));
      const float inv = fminf(1.f + __builtin_amdgcn_exp2f(L2E * lg[a]), 1e30f);
      unsigned h2[7][2];
#pragma unroll
      for (int m = 0; m < 7; ++m) {
        float h[4];
#pragma unroll
        for (int r = 0; r < 4; ++r) {
          const float e = __builtin_amdgcn_exp2f(-L2E * hg[a][m][r]);
          h[r] = hu[a][m][r] * __builtin_amdgcn_rcpf(__builtin_fmaf(e, inv, inv));
        }
        h2[m][0] = pk2(h[0], h[1]);
        h2[m][1] = pk2(h[2], h[3]);
      }
#pragma unroll
      for (int v = 0; v < 3; ++v) {
        const u32x4 d = {h2[2 * v][0], h2[2 * v][1], h2[2 * v + 1][0], h2[2 * v + 1][1]};
        __builtin_amdgcn_raw_buffer_store_b128(d, rWs, hp + v * 8192 + a * 1024, 0, AUX_SC1);
      }
      const u32x2 d2 = {h2[6][0], h2[6][1]};
      __builtin_amdgcn_raw_buffer_store_b64(d2, rWs, hs + a * 1024, 0, AUX_SC1);
    }
  }

  f32x4 acc[2][7];
#pragma unroll
  for (int a = 0; a < 2; ++a)
#pragma unroll
    for (int m = 0; m < 7; ++m) acc[a][m] = f32x4{0, 0, 0, 0};
  {
    const int l3 = late(lane), g = l3 >> 4, c = l3 & 15;
    const int oT0 = (32 * cw + c) * 128 + ((g ^ (c >> 1)) << 4), oT1 = oT0 ^ 64;
    const int oE = 16384 + l3 * 16;
    const int oH = 2 * cw * 1024 + l3 * 16;
    const int oD = 16384 + (4 * g + (c >> 2)) * 224 + 8 * (c & 3);
    const unsigned myflag2 = WS_FLAG2 + ((I * 4 + q) * 4 + j) * 8u;
    bf16x8 ft[2][2], fe[2][7];
    auto rd = [&](auto N, auto H) {
      constexpr int n = N.value, h = H.value;
      LAS char* slot = lds + st_off<n>();
      if constexpr (n < N4) {
#pragma unroll
        for (int a = 0; a < 2; ++a) ft[h][a] = ldsr(slot + (h ? oT1 : oT0) + a * 2048);
#pragma unroll
        for (int m = 0; m < 7; ++m) fe[h][m] = ldsr(slot + oE + h * 7168 + m * 1024);
      } else {
#pragma unroll
        for (int a = 0; a < 2; ++a) ft[h][a] = ldsr(slot + oH + h * 8192 + a * 1024);
#pragma unroll
        for (int m = 0; m < 7; ++m) {
          LAS char* p = slot + oD + h * 7168 + m * 32;
          fe[h][m] = ldstr(p, p + 3584);
        }
      }
    };
    auto mm = [&](auto H) {
      constexpr int h = H.value;
#pragma unroll
      for (int m = 0; m < 7; ++m)
#pragma unroll
        for (int a = 0; a < 2; ++a) acc[a][m] = mfma(fe[h][m], ft[h][a], acc[a][m]);
    };
    rd(IC<N3>{}, IC<0>{});
    static_for<N3, NST>([&](auto N) {
      constexpr int n = N.value;
      rd(N, IC<1>{});
      mm(IC<0>{});
      interleave<(n < N4 ? 9 : 16), 14>();
      SBAR();
      if constexpr (n + 1 < NST) {
        WAITLGKM0();
        if constexpr (n + 1 == B_FLAG2) WAITVM(0);
        BAR();
        if constexpr (n + 1 == B_FLAG2) { if (cw == 0 && lane == 0) st_flag(ws, myflag2, tag); }
        rd(IC<n + 1>{}, IC<0>{});
      }
      mm(IC<1>{});
      if constexpr (n + 1 < NST) interleave<(n + 1 < N4 ? 9 : 16), 14>();
      SBAR();
      if constexpr (n + 1 == N4) {
#pragma unroll
        for (int a = 0; a < 2; ++a)
#pragma unroll
          for (int m = 0; m < 7; ++m) acc[a][m] *= gs[a];
        SBAR();
      }
    });
  }

  const int l4 = late(lane), g = l4 >> 4, c = l4 & 15;
  const unsigned inb = WS_INBOX + b * 32768u + cw * 2048u + l4 * 16u;
  f32x4 own[4];
#pragma unroll
  for (int k = 0; k < 4; ++k) own[k] = f32x4{0, 0, 0, 0};
  static_for<0, 4>([&](auto Q) {
    constexpr int qd = Q.value, a = qd & 1, mb = (qd >> 1) * 4;
    if (qd != q) {
      unsigned d[8];
#pragma unroll
      for (int k = 0; k < 4; ++k) {
        if (mb + k < 7) {
          const f32x4 x = acc[a][mb + k < 7 ? mb + k : 0];
          const unsigned u0 = pk2(x[0], x[1]), u1 = pk2(x[2], x[3]);
          d[2 * k] = u0 < SENT ? u0 : SENT - 1;
          d[2 * k + 1] = u1 < SENT ? u1 : SENT - 1;
        } else { d[2 * k] = 0; d[2 * k + 1] = 0; }
      }
      const unsigned o = WS_INBOX + ((b & ~3) | qd) * 32768u + q * 8192u + cw * 2048u + l4 * 16u;
      __builtin_amdgcn_raw_buffer_store_b128(u32x4{d[0], d[1], d[2], d[3]}, rWs, o, 0, AUX_SC1);
      __builtin_amdgcn_raw_buffer_store_b128(u32x4{d[4], d[5], d[6], d[7]}, rWs, o + 1024, 0, AUX_SC1);
    } else {
#pragma unroll
      for (int k = 0; k < 4; ++k) if (mb + k < 7) own[k] = acc[a][mb + k < 7 ? mb + k : 0];
    }
  });
  u32x4 in[3][2];
  for (int it = 0; it < SPIN_MAX; ++it) {
    bool miss = false;
#pragma unroll
    for (int d = 0; d < 3; ++d) {
      const unsigned o = inb + ((q + d + 1) & 3) * 8192u;
      in[d][0] = __builtin_amdgcn_raw_buffer_load_b128(rWs, o, 0, AUX_SC1);
      in[d][1] = __builtin_amdgcn_raw_buffer_load_b128(rWs, o + 1024, 0, AUX_SC1);
    }
#pragma unroll
    for (int d = 0; d < 3; ++d)
#pragma unroll
      for (int h = 0; h < 2; ++h)
#pragma unroll
        for (int k = 0; k < 4; ++k) miss |= in[d][h][k] == SENT;
    if (__builtin_amdgcn_ballot_w64(miss) == 0) break;
    asm volatile("" ::: "memory");
  }
  {
    const rsrc_t rO = mkrs(out, 2048u * 896u);
    const int mb = (q >> 1) * 4;
    const unsigned ob = (128 * I + 32 * cw + 16 * (q & 1) + c) * 896u + (112 * j + 16 * mb + 4 * g) * 2u;
#pragma unroll
    for (int k = 0; k < 4; ++k) {
      f32x4 s = own[k];
#pragma unroll
      for (int d = 0; d < 3; ++d) {
        const unsigned u0 = in[d][k >> 1][2 * (k & 1)], u1 = in[d][k >> 1][2 * (k & 1) + 1];
        s += f32x4{bflo(u0), bfhi(u0), bflo(u1), bfhi(u1)};
      }
      if (k < 3 || mb == 0) {
        const u32x2 o2 = {pk2(s[0], s[1]), pk2(s[2], s[3])};
        __builtin_amdgcn_raw_buffer_store_b64(o2, rO, ob + 32 * k, 0, 0);
      }
    }
  }
}

__global__ __launch_bounds__(512, 2) void fused_kernel(const void* Ax, const void* Cre, const void* Cim, const void* Wdre, const void* Wdim,
                                                       const void* Wg, const void* Wu, const void* Wdn, const void* gw, void* out, char* ws) {
  __shared__ __attribute__((aligned(1024))) char shm[LDS_BYTES];
  LAS char* lds = (LAS char*)shm;
  {
    SBAR();
    u64 pc;
    unsigned len, o0, o1, oc;
    asm volatile("s_getpc_b64 %0\n.Lfk_entry:\n\ts_mov_b32 %1, .Lfk_code_end-.Lfk_entry\n\ts_mov_b32 %2, .Lfk_loader0-.Lfk_entry-256\n\t"
                 "s_mov_b32 %3, .Lfk_loader1-.Lfk_entry-256\n\ts_mov_b32 %4, .Lfk_loaders-.Lfk_entry"
                 : "=s"(pc), "=s"(len), "=s"(o0), "=s"(o1), "=s"(oc));
    const unsigned w = threadIdx.x >> 6, l = threadIdx.x & 63;
    const unsigned o = w < 2 && l < 36 ? (l < 4 ? oc : w ? o1 : o0) + l * 64u : 0x80000000u;
    __builtin_amdgcn_raw_ptr_buffer_load_lds(mkrs((const void*)pc, len), (LAS void*)(lds + PF_OFF), 4, o, 0, 0, 0);
    SBAR();
  }
  asm volatile("" : "+s"(Ax), "+s"(Cre), "+s"(Cim), "+s"(Wdre), "+s"(Wdim), "+s"(Wg), "+s"(Wu), "+s"(Wdn), "+s"(gw), "+s"(out), "+s"(ws));
  Geo G;
  G.lane = threadIdx.x & 63;
  G.wid = __builtin_amdgcn_readfirstlane(threadIdx.x >> 6);
  G.b = blockIdx.x;
  G.q = G.b & 3;
  G.r = (G.b >> 2) & 1;
  G.j = (G.b >> 3) & 3;
  G.i = G.b >> 5;
  G.I = 8 * G.r + G.i;
  G.k7 = (G.i + 2 * G.j) % 7;
  G.k16 = 2 * G.i;
  if (G.wid >= 4) {
    asm volatile(".Lfk_loaders:");
    SBAR();
    const bool im = G.q >= 2;
    const void* Cp = im ? Cim : Cre;
    const void* Wdp = im ? Wdim : Wdre;
    if (G.wid < 6) loader<0>(G, lds, ws, Ax, Cp, Wdp, Wg, Wu, Wdn);
    else loader<1>(G, lds, ws, Ax, Cp, Wdp, Wg, Wu, Wdn);
  } else {
    compute(G, lds, ws, gw, out, Wg, Wu, Wdn, G.q >= 2 ? Cim : Cre);
  }
  asm volatile(".Lfk_code_end:");
}

}

extern "C" void kernel_launch(void* const* d_in, const int* in_sizes, int n_in, void* d_out, int out_size, void* d_ws,
                              size_t ws_size, hipStream_t stream) {
  (void)in_sizes; (void)n_in; (void)out_size; (void)ws_size;
  fk::fused_kernel<<<256, 512, 0, stream>>>(d_in[0], d_in[1], d_in[2], d_in[3], d_in[4], d_in[6], d_in[7], d_in[8], d_in[9],
                                            d_out, (char*)d_ws);
}
```

```cpp
#include <hip/hip_runtime.h>
#include <hip/hip_bf16.h>
#include <stdint.h>

#define LAS __attribute__((address_space(3)))
#define GAS __attribute__((address_space(1)))

namespace fk {

typedef __bf16 bf16x2 __attribute__((ext_vector_type(2)));
typedef __bf16 bf16x4 __attribute__((ext_vector_type(4)));
typedef __bf16 bf16x8 __attribute__((ext_vector_type(8)));
typedef float f32x2 __attribute__((ext_vector_type(2)));
typedef float f32x4 __attribute__((ext_vector_type(4)));
typedef unsigned u32x2 __attribute__((ext_vector_type(2)));
typedef unsigned u32x4 __attribute__((ext_vector_type(4)));
typedef unsigned long long u64;
typedef __amdgpu_buffer_rsrc_t rsrc_t;

template <int V> struct IC { static constexpr int value = V; };
template <int B, int E, class Fn> __device__ __forceinline__ void static_for(Fn&& f) {
  if constexpr (B < E) { f(IC<B>{}); static_for<B + 1, E>(f); }
}

constexpr int NST = 37, N2 = 7, N3 = 14, N4 = 30;
constexpr int SZ1 = 22528, SZ2 = 45056, SZ3 = 30720;
constexpr int RING = 161792, GW_OFF = RING, PF_OFF = RING + 1024, LDS_BYTES = RING + 2048;
constexpr int st_size(int n) { return n < N2 ? SZ1 : (n < N3 ? SZ2 : SZ3); }
constexpr int st_szA(int n) { return n < N2 ? SZ1 : 16384; }
constexpr int cntA(int cls, int n) { return n < N2 ? (cls ? 5 : 6) : 4; }
constexpr int cntB(int cls, int n) { return n < N2 ? 0 : (n < N3 ? 7 : (cls ? 3 : 4)); }
constexpr int N_GAP = 19, T_GAP = 17;
struct Plan { int off[NST]; int atA[NST]; int atB[NST]; };
constexpr Plan make_plan() {
  Plan p{};
  constexpr int ova[10] = {-1, -1, 0, 0, 1, 2, 3, 4, 5, 7};
  constexpr int ovb[3] = {5, 6, 8};
  int cur = 0;
  for (int n = 0; n < NST; ++n) {
    if (cur + st_size(n) > RING) cur = 0;
    p.off[n] = cur;
    cur += st_size(n);
    int a = -1, b = -1;
    const int lo = p.off[n], mid = lo + st_szA(n), hi = lo + st_size(n);
    for (int m = 0; m < n; ++m) {
      const int ml = p.off[m], mh = ml + st_size(m);
      if (ml < mid && lo < mh) a = m + 1;
      if (mid < hi && ml < hi && mid < mh) b = m + 1;
    }
    if (n < 10 && a < ova[n]) a = ova[n];
    if (n >= N2 && n < 10 && b < ovb[n - N2]) b = ovb[n - N2];
    if (n >= 10) { if (a < b) a = b; b = a; }
    if (n > 0 && a < p.atA[n - 1]) a = p.atA[n - 1];
    if (n > N2 && b < p.atB[n - 1]) b = p.atB[n - 1];
    if (n == N_GAP && a < T_GAP) a = T_GAP;
    if ((n > N_GAP || (n > N3 && n < N3 + 3)) && a <= p.atA[n - 1]) a = p.atA[n - 1] + 1;
    if (n >= 10 && b < a) b = a;
    if (n >= 10 && a < b) a = b;
    p.atA[n] = a;
    p.atB[n] = n < N2 ? a : b;
  }
  return p;
}
constexpr Plan PLAN = make_plan();
template <int N> constexpr int st_off() { return PLAN.off[N]; }
template <int N> constexpr int st_atA() { return PLAN.atA[N]; }
template <int N> constexpr int st_atB() { return PLAN.atB[N]; }
constexpr int T_EPOCH = -1;
constexpr int T_PF1 = PLAN.atA[N3] - 2, T_PF2 = PLAN.atA[N4] - 2;
constexpr int T_C0 = 7, T_C1 = 9, T_C2 = 11, T_C3 = 13;
constexpr int T_D = 20;
constexpr int pf_cnt(int a) {
  return (a == T_C0) + (a == T_C1) + (a == T_C2) + (a == T_C3) + (a == T_C0 + 1) + (a == T_C1 + 1) + (a == T_C2 + 1) + (a == T_C3 + 1) +
         (a == T_D) + (a == T_EPOCH) + (a == T_PF1) + (a == T_PF2);
}
constexpr int st_wait_c(int cls, int n) {
  int c = 0;
  bool seenA = false, seenB = false;
  for (int t = -1; t <= n - 1; ++t) {
    for (int s = 0; s < NST; ++s) {
      if (PLAN.atA[s] == t) { if (s == n) { seenA = true; c = 0; } else c += cntA(cls, s); }
      if (PLAN.atB[s] == t && cntB(cls, s) > 0) { if (s == n) { seenB = true; c = 0; } else c += cntB(cls, s); }
    }
    c += pf_cnt(t);
  }
  return seenA && (seenB || cntB(cls, n) == 0) ? c : -1;
}
template <int CLS, int N> constexpr int st_wait() { return st_wait_c(CLS, N); }
constexpr bool plan_ok() {
  for (int n = 0; n < NST; ++n)
    for (int cls = 0; cls < 2; ++cls)
      if (st_wait_c(cls, n) < 0 || st_wait_c(cls, n) > 62) return false;
  return true;
}
static_assert(plan_ok(), "stage plan");

constexpr int B_TAG = 4;
constexpr int B_FLAG1 = 9;
constexpr int N_POLL1 = 11;
constexpr int B_FLAG2 = 21;

constexpr unsigned WS_EPOCH = 0;
constexpr unsigned WS_FLAG1 = 4096;
constexpr unsigned WS_FLAG2 = 8192;
constexpr unsigned WS_LOGIT = 16384;
constexpr unsigned WS_ABT = 131072;
constexpr unsigned WS_H = 4u << 20;
constexpr unsigned WS_INBOX = 12u << 20;
constexpr unsigned WS_END = 20u << 20;
constexpr unsigned SENT = 0xFFFFFFFFu;
constexpr int SPIN_MAX = 1 << 20;
constexpr int AUX_SC1 = 16;

#define BAR() do { asm volatile("" ::: "memory"); __builtin_amdgcn_s_barrier(); asm volatile("" ::: "memory"); } while (0)
#define WAITVM(n) asm volatile("s_waitcnt vmcnt(%0)" ::"n"(n) : "memory")
#define WAITLGKM0() do { __builtin_amdgcn_s_waitcnt(0xC07F); asm volatile("" ::: "memory"); } while (0)
#define SBAR() __builtin_amdgcn_sched_barrier(0)

__device__ __forceinline__ rsrc_t mkrs(const void* p, unsigned bytes) {
  return __builtin_amdgcn_make_buffer_rsrc((void*)p, 0, (int)bytes, 0x00020000);
}
template <int AUX> __device__ __forceinline__ void dma(rsrc_t rs, unsigned voff, unsigned soff, LAS char* l) {
  __builtin_amdgcn_raw_ptr_buffer_load_lds(rs, (LAS void*)l, 16, voff, soff, 0, AUX);
}
__device__ __forceinline__ bf16x8 ldsr(LAS char* p) { return *(LAS bf16x8*)p; }
__device__ __forceinline__ bf16x8 ldstr(LAS char* p0, LAS char* p1) {
  bf16x4 a = __builtin_amdgcn_ds_read_tr16_b64_v4bf16((LAS bf16x4*)p0);
  bf16x4 b = __builtin_amdgcn_ds_read_tr16_b64_v4bf16((LAS bf16x4*)p1);
  return __builtin_shufflevector(a, b, 0, 1, 2, 3, 4, 5, 6, 7);
}
__device__ __forceinline__ f32x4 mfma(bf16x8 a, bf16x8 b, f32x4 c) { return __builtin_amdgcn_mfma_f32_16x16x32_bf16(a, b, c, 0, 0, 0); }
__device__ __forceinline__ unsigned pk2(float x, float y) {
  f32x2 v = {x, y};
  return __builtin_bit_cast(unsigned, __builtin_convertvector(v, bf16x2));
}
__device__ __forceinline__ float bflo(unsigned d) { return __builtin_bit_cast(float, d << 16); }
__device__ __forceinline__ float bfhi(unsigned d) { return __builtin_bit_cast(float, d & 0xFFFF0000u); }
__device__ __forceinline__ u64 ld_flag(const char* ws, unsigned off) {
  return __hip_atomic_load((const GAS u64*)(ws + off), __ATOMIC_RELAXED, __HIP_MEMORY_SCOPE_AGENT);
}
__device__ __forceinline__ void st_flag(char* ws, unsigned off, u64 v) {
  __hip_atomic_store((GAS u64*)(ws + off), v, __ATOMIC_RELAXED, __HIP_MEMORY_SCOPE_AGENT);
}
__device__ __forceinline__ u64 make_tag(u64 epoch) {
  const unsigned lo = __builtin_amdgcn_readfirstlane((unsigned)epoch), hi = __builtin_amdgcn_readfirstlane((unsigned)(epoch >> 32));
  return (((u64)hi << 32) | lo) * 6364136223846793005ull + 1442695040888963407ull;
}
__device__ __forceinline__ void wait_flag(const char* ws, unsigned off, u64 v, u64 tag) {
  asm volatile("" : "+v"(v));
  if (__builtin_amdgcn_ballot_w64(v != tag) != 0) {
    for (int it = 0; it < SPIN_MAX; ++it) {
      __builtin_amdgcn_s_sleep(2);
      if (__builtin_amdgcn_ballot_w64(ld_flag(ws, off) != tag) == 0) break;
    }
  }
  asm volatile("" ::: "memory");
}
template <int NR, int NM> __device__ __forceinline__ void interleave() {
  static_for<0, NM>([&](auto K) {
    constexpr int k = K.value, cnt = NR / NM + (k < NR % NM ? 1 : 0);
    __builtin_amdgcn_sched_group_barrier(0x008, 1, 0);
    if constexpr (cnt > 0) __builtin_amdgcn_sched_group_barrier(0x100, cnt, 0);
  });
}

__device__ __forceinline__ int late(int x) { asm volatile("" : "+v"(x)); return x; }

struct Geo { int b, r, q, i, j, I, lane, wid, k7, k16; };
template <int NK> __device__ __forceinline__ unsigned kstep(const Geo& G, int k) {
  const unsigned x = (NK == 7 ? G.k7 : G.k16) + k;
  return x >= NK ? x - NK : x;
}

template <int CLS>
__device__ __forceinline__ void loader(const Geo& G, LAS char* lds, char* ws, const void* Ax, const void* Cp, const void* Wdp,
                                       const void* Wg, const void* Wu, const void* Wdn) {
  const int lane = G.lane, lw = G.wid - 4, q = G.q, j = G.j, I = G.I;
  asm volatile(".Lfk_loader%0:" ::"n"(CLS));
  SBAR();
  const rsrc_t rA = mkrs(Ax, 2048u * 896u), rWd = mkrs(Wdp, 448u * 896u);
  constexpr int NB = CLS ? 3 : 4;
  const int blk0 = CLS ? 8 + 3 * (lw - 2) : 4 * lw;
  unsigned vA[2], vWd[NB], vX[4], vW[7], vC[4], vWdn[NB];
  {
    const int r8 = lane >> 3, ch = lane & 7;
    const int sig = 2 * I + (q & 1);
#pragma unroll
    for (int n = 0; n < 2; ++n) {
      const int row = 16 * lw + 8 * n + r8;
      const int f = ((row >> 1) & 1) | (((row >> 3) & 3) << 1);
      vA[n] = (64 * sig + row) * 896u + ((ch ^ f) << 4);
    }
#pragma unroll
    for (int n = 0; n < NB; ++n) {
      const int row = 8 * (blk0 + n) + r8;
      vWd[n] = (112 * j + row) * 896u + ((ch ^ ((row >> 1) & 7)) << 4);
    }
  }
  const rsrc_t rC = mkrs(Cp, 2048u * 4096u), rWs = mkrs(ws, WS_END), rWdn = mkrs(Wdn, 1792u * 896u);
  const rsrc_t rW = mkrs(lw < 2 ? Wg : Wu, 448u * 3584u);
  unsigned vAB, vH, fo1, fo2, tD, tC, tK;
  u64 tag = 0, f1 = 0, f2 = 0;

  auto issueA = [&](auto S) {
    constexpr int s = S.value;
    LAS char* slot = lds + st_off<s>();
    if constexpr (s < N2) {
#pragma unroll
      for (int n = 0; n < 2; ++n) dma<0>(rA, vA[n], 128 * kstep<7>(G, s), slot + (2 * lw + n) * 1024);
#pragma unroll
      for (int n = 0; n < NB; ++n) dma<0>(rWd, vWd[n], 128 * kstep<7>(G, s), slot + 8192 + (blk0 + n) * 1024);
    } else if constexpr (s < N3) {
      const unsigned k = kstep<7>(G, s - N2);
#pragma unroll
      for (int n = 0; n < 4; ++n) dma<0>(rA, vX[n], 128 * k, slot + (4 * lw + n) * 1024);
    } else if constexpr (s < N4) {
      const unsigned k = kstep<16>(G, s - N3);
      if constexpr (s == N3) wait_flag(ws, fo1, f1, tag);
#pragma unroll
      for (int n = 0; n < 4; ++n) dma<0>(rC, vC[n], 128 * k, slot + (4 * lw + n) * 1024);
    } else {
      const unsigned k = kstep<7>(G, s - N4);
      if constexpr (s == N4) wait_flag(ws, fo2, f2, tag);
#pragma unroll
      for (int n = 0; n < 4; ++n) dma<AUX_SC1>(rWs, vH, 16384 * k + 1024 * n, slot + (4 * lw + n) * 1024);
    }
  };
  auto issueB = [&](auto S) {
    constexpr int s = S.value;
    LAS char* slot = lds + st_off<s>();
    if constexpr (s >= N2 && s < N3) {
      const unsigned k = kstep<7>(G, s - N2);
#pragma unroll
      for (int n = 0; n < 7; ++n) dma<0>(rW, vW[n], 229376 * k, slot + 16384 + (7 * lw + n) * 1024);
    } else if constexpr (s >= N3 && s < N4) {
      const unsigned k = kstep<16>(G, s - N3);
#pragma unroll
      for (int n = 0; n < NB; ++n) dma<AUX_SC1>(rWs, vAB, 14336 * k + 1024 * n, slot + 16384 + (blk0 + n) * 1024);
    } else if constexpr (s >= N4) {
      const unsigned k = kstep<7>(G, s - N4);
#pragma unroll
      for (int n = 0; n < NB; ++n) dma<0>(rWdn, vWdn[n], 57344 * k, slot + 16384 + (blk0 + n) * 1024);
    }
  };
  auto issue_at = [&](auto T) {
    static_for<0, NST>([&](auto S) {
      if constexpr (st_atA<S.value>() == T.value) issueA(S);
      if constexpr (st_atB<S.value>() == T.value) issueB(S);
    });
  };
  auto touch = [&](rsrc_t rs, unsigned voff, unsigned soff) {
    __builtin_amdgcn_raw_ptr_buffer_load_lds(rs, (LAS void*)(lds + PF_OFF), 4, voff, soff, 0, 0);
  };
  auto vpf = [&](auto A) {
    constexpr int a = A.value;
    auto touchC = [&](int e, int par) {
      const unsigned o = tC + (((tK + 4 * e) & 15) << 7);
      touch(rC, (G.i & 1) == par ? o : 0x80000000u, 0);
    };
    if constexpr (a == T_C0) touchC(0, 0);
    if constexpr (a == T_C1) touchC(1, 0);
    if constexpr (a == T_C2) touchC(2, 0);
    if constexpr (a == T_C3) touchC(3, 0);
    if constexpr (a == T_C0 + 1) touchC(0, 1);
    if constexpr (a == T_C1 + 1) touchC(1, 1);
    if constexpr (a == T_C2 + 1) touchC(2, 1);
    if constexpr (a == T_C3 + 1) touchC(3, 1);
    if constexpr (a == T_D) touch(rWdn, tD, 0);
    if constexpr (a == T_PF1) f1 = ld_flag(ws, fo1);
    if constexpr (a == T_PF2) f2 = ld_flag(ws, fo2);
  };
  issue_at(IC<-1>{});
  const int ln = late(lane);
  u64 epoch = ld_flag(ws, WS_EPOCH);
  {
    const int r8 = ln >> 3, ch = ln & 7;
#pragma unroll
    for (int n = 0; n < 4; ++n) {
      const int row = 32 * lw + 8 * n + r8;
      const unsigned sw = (ch ^ ((row >> 1) & 7)) << 4;
      vX[n] = (128 * I + row) * 896u + sw;
      vC[n] = (128 * I + row) * 4096u + sw + 2048u * (q & 1);
    }
#pragma unroll
    for (int n = 0; n < 7; ++n) {
      const int idx = 7 * lw + n, ii = idx >= 14 ? idx - 14 : idx;
      const unsigned c16 = ii * 64 + ln, pr = (c16 * 4682u) >> 16, cc = c16 - 14u * pr;
      const unsigned rho = (pr & ~12u) | ((pr & 8) >> 1) | ((pr & 4) << 1);
      vW[n] = rho * 3584u + cc * 16u + (448u * q + 112u * j) * 2u;
    }
#pragma unroll
    for (int n = 0; n < NB; ++n) {
      const unsigned c16 = (blk0 + n) * 64 + ln, pr = (c16 * 4682u) >> 16, cc = c16 - 14u * pr;
      vWdn[n] = (448 * q + pr) * 896u + 224u * j + cc * 16u;
    }
    vAB = WS_ABT + (q * 4 + j) * 16u * 14336u + blk0 * 1024u + ln * 16u;
    vH = WS_H + (I * 4 + q) * 114688u + lw * 4096u + ln * 16u;
    fo1 = WS_FLAG1 + ((q * 4 + j) * 16 + (ln & 15)) * 8u;
    fo2 = WS_FLAG2 + ((I * 4 + q) * 4 + (ln & 3)) * 8u;
    const unsigned pid = ((G.i * 4 + j) * 4 + lw) * 49 + ln;
    tD = ln < 49 ? 448u * 896u * q + pid * 64u : 0x80000000u;
    tC = (128 * I + 32 * j + 8 * lw + ((ln >> 1) & 7)) * 4096u + 2048u * (q & 1) + (ln & 1) * 64u;
    tK = kstep<16>(G, 0) + (ln >> 4);
  }
  vpf(IC<-1>{});
  static_for<0, NST>([&](auto N) {
    constexpr int n = N.value;
    WAITVM((st_wait<CLS, n>()));
    BAR();
    if constexpr (n == B_TAG) {
      asm volatile("" : "+v"(epoch));
      tag = make_tag(epoch);
    }
    issue_at(N);
    vpf(N);
  });
  if (G.b == 0 && lw == 3) {
#pragma unroll
    for (int k = 0; k < 4; ++k) {
      const unsigned fo = WS_FLAG1 + (k * 64 + lane) * 8u;
      wait_flag(ws, fo, ld_flag(ws, fo), tag);
    }
    if (lane == 0) st_flag(ws, WS_EPOCH, tag);
  }
}

__device__ __forceinline__ void clear_inbox(const Geo& G, rsrc_t rWs) {
#pragma unroll
  for (int d = 1; d < 4; ++d) {
    const u32x4 s4 = {SENT, SENT, SENT, SENT};
    const unsigned o = WS_INBOX + G.b * 32768u + G.wid * 2048u + late(G.lane) * 16u + ((G.q + d) & 3) * 8192u;
    __builtin_amdgcn_raw_buffer_store_b128(s4, rWs, o, 0, AUX_SC1);
    __builtin_amdgcn_raw_buffer_store_b128(s4, rWs, o + 1024, 0, AUX_SC1);
  }
}
template <int EH>
__device__ __forceinline__ void g1_phase(const Geo& G, LAS char* lds, char* ws, rsrc_t rWs) {
  const int lane = G.lane, cw = G.wid, g = lane >> 4, c = lane & 15, sh = cw >> 1;
  f32x4 acc[2][4];
#pragma unroll
  for (int a = 0; a < 2; ++a)
#pragma unroll
    for (int k = 0; k < 4; ++k) acc[a][k] = f32x4{0, 0, 0, 0};
  const int fA = ((c >> 1) & 1) | ((c >> 2) << 1);
  const int oA0 = (32 * sh + 8 * (c >> 2) + (c & 3)) * 128 + ((g ^ fA) << 4), oA1 = oA0 ^ 64;
  const int oB0 = 8192 + (64 * EH + c) * 128 + ((g ^ (c >> 1)) << 4), oB1 = oB0 ^ 64;
  const int oG = GW_OFF + 16 * g;
  bf16x8 fa[2][2], fb[2][4];
  auto rd = [&](auto N, auto H) {
    constexpr int n = N.value, h = H.value;
    LAS char* slot = lds + st_off<n>();
#pragma unroll
    for (int a = 0; a < 2; ++a) fa[h][a] = ldsr(slot + (h ? oA1 : oA0) + a * 512);
#pragma unroll
    for (int k = 0; k < 4; ++k) {
      if (EH && k == 3) fb[h][k] = ldsr(lds + (oG + 128 * kstep<7>(G, n)) + 64 * h);
      else fb[h][k] = ldsr(slot + (h ? oB1 : oB0) + k * 2048);
    }
  };
  auto mm = [&](auto H) {
    constexpr int h = H.value;
#pragma unroll
    for (int k = 0; k < 4; ++k)
#pragma unroll
      for (int a = 0; a < 2; ++a) acc[a][k] = mfma(fa[h][a], fb[h][k], acc[a][k]);
  };
  BAR();
  rd(IC<0>{}, IC<0>{});
  static_for<0, N2>([&](auto N) {
    constexpr int n = N.value;
    rd(N, IC<1>{});
    mm(IC<0>{});
    interleave<6, 8>();
    SBAR();
    WAITLGKM0();
    BAR();
    if constexpr (n == 0) clear_inbox(G, rWs);
    if constexpr (n + 1 < N2) rd(IC<n + 1>{}, IC<0>{});
    mm(IC<1>{});
    if constexpr (n + 1 < N2) interleave<6, 8>();
    SBAR();
  });
  const int p = G.q >> 1;
  const unsigned base = WS_ABT + (((2 * p + G.r) * 4 + G.j) * 16 + 2 * G.i + (G.q & 1)) * 14336u + sh * 7168u + EH * 4096u + lane * 16u;
  const float sg = p ? -1.f : 1.f;
#pragma unroll
  for (int k = 0; k < (EH ? 3 : 4); ++k) {
    const f32x4 x = acc[0][k] * sg, y = acc[1][k] * sg;
    const u32x4 v = {pk2(x[0], x[1]), pk2(x[2], x[3]), pk2(y[0], y[1]), pk2(y[2], y[3])};
    __builtin_amdgcn_raw_buffer_store_b128(v, rWs, base + k * 1024, 0, AUX_SC1);
  }
  if (EH && c == 0) {
    const unsigned lo = WS_LOGIT + G.b * 256u + (32 * sh + 8 * g) * 4u;
    __builtin_amdgcn_raw_buffer_store_b128(__builtin_bit_cast(u32x4, acc[0][3]), rWs, lo, 0, AUX_SC1);
    __builtin_amdgcn_raw_buffer_store_b128(__builtin_bit_cast(u32x4, acc[1][3]), rWs, lo + 16, 0, AUX_SC1);
  }
}

__device__ __forceinline__ void compute(const Geo& G, LAS char* lds, char* ws, const void* gw, void* out, const void* Wg,
                                        const void* Wu, const void* Wdn, const void* Cp) {
  const int lane = G.lane, cw = G.wid, q = G.q, j = G.j, I = G.I, b = G.b;
  const u64 epoch = ld_flag(ws, WS_EPOCH);
  const rsrc_t rWs = mkrs(ws, WS_END);
  {
    u64 pc;
    unsigned len;
    asm volatile("s_getpc_b64 %0\n.Lfk_code_begin:\n\ts_mov_b32 %1, .Lfk_code_end-.Lfk_code_begin" : "=s"(pc), "=s"(len));
    const unsigned o = lane < 7 ? (((b >> 3) * 4 + cw) * 7 + lane) * 64u : 0x80000000u;
    __builtin_amdgcn_raw_ptr_buffer_load_lds(mkrs((const void*)pc, len), (LAS void*)(lds + PF_OFF), 4, o, 0, 0, 0);
  }
  if (cw == 2) {
    const unsigned last = lane & 1;
    const char* pa = (const char*)Wg + last * (448u * 3584u - 4);
    if ((lane >> 1) == 1) pa = (const char*)Wu + last * (448u * 3584u - 4);
    if ((lane >> 1) == 2) pa = (const char*)Wdn + 401408u * q + last * 401404u;
    if ((lane >> 1) == 3) pa = (const char*)Cp + 524288u * I + last * 524284u;
    if (lane < 8) __builtin_amdgcn_global_load_lds((const GAS void*)pa, (LAS void*)(lds + PF_OFF + 512), 4, 0, 0);
  }
  if (cw == 1) {
    const rsrc_t rG = mkrs(gw, 896u);
    const u32x4 v = __builtin_amdgcn_raw_buffer_load_b128(rG, lane * 16u, 0, 0);
    *(LAS u32x4*)(lds + GW_OFF + lane * 16) = v;
    WAITLGKM0();
  }
  __builtin_amdgcn_s_waitcnt(0x0F70);
  const u64 tag = make_tag(epoch);
  if (cw & 1) g1_phase<1>(G, lds, ws, rWs);
  else g1_phase<0>(G, lds, ws, rWs);

  const unsigned myflag1 = WS_FLAG1 + (((2 * (q >> 1) + G.r) * 4 + j) * 16 + 2 * G.i + (q & 1)) * 8u;
  float lg[2];
  f32x4 hg[2][7], hu[2][7];
#pragma unroll
  for (int a = 0; a < 2; ++a)
#pragma unroll
    for (int m = 0; m < 7; ++m) { hg[a][m] = f32x4{0, 0, 0, 0}; hu[a][m] = f32x4{0, 0, 0, 0}; }
  {
    const int l2 = late(lane), g = l2 >> 4, c = l2 & 15;
    const int oT0 = (32 * cw + c) * 128 + ((g ^ (c >> 1)) << 4), oT1 = oT0 ^ 64;
    const int oWb = 16384 + (16 * (g >> 1) + 4 * (g & 1) + (c >> 2)) * 224 + 8 * (c & 3);
    const int oWP = oWb + 32 * (j & 1), oWS = oWb + ((j & 1) ? 0 : 192);
    bf16x8 t[2][2], wg[3], wu[3];
    u64 fv = 0;
    auto rdW = [&](auto N, auto U) {
      constexpr int n = N.value, u = U.value, kap = u / 7, m = u % 7, bi = (14 * (n - N2) + u) % 3;
      LAS char* p = lds + st_off<n>() + (m < 6 ? oWP + 32 * m : oWS) + kap * 7168;
      wg[bi] = ldstr(p, p + 1792);
      wu[bi] = ldstr(p + 14336, p + 14336 + 1792);
    };
    auto rdT = [&](auto N, auto H) {
      constexpr int n = N.value, h = H.value;
#pragma unroll
      for (int a = 0; a < 2; ++a) t[h][a] = ldsr(lds + st_off<n>() + (h ? oT1 : oT0) + a * 2048);
    };
    rdT(IC<N2>{}, IC<0>{});
    rdW(IC<N2>{}, IC<0>{});
    rdW(IC<N2>{}, IC<1>{});
    SBAR();
    static_for<N2, N3>([&](auto N) {
      constexpr int n = N.value;
      if constexpr (n == N_POLL1 || n == N_POLL1 + 1) {
        const int lp = late(lane), qq = lp & 3;
        const unsigned fo4 = WS_FLAG1 + (((2 * (qq >> 1) + G.r) * 4 + j) * 16 + 2 * G.i + (qq & 1)) * 8u;
        if constexpr (n == N_POLL1) fv = ld_flag(ws, fo4);
        else wait_flag(ws, fo4, fv, tag);
      }
      if constexpr (n == N_POLL1 + 1) {
        const unsigned lgo = WS_LOGIT + ((b & ~1) | (cw >> 1)) * 256u + (32 * (cw & 1) + (late(lane) & 15)) * 4u;
#pragma unroll
        for (int a = 0; a < 2; ++a) lg[a] = __builtin_bit_cast(float, __builtin_amdgcn_raw_buffer_load_b32(rWs, lgo + 64 * a, 0, AUX_SC1));
      }
      static_for<0, 14>([&](auto U) {
        constexpr int u = U.value, kap = u / 7, m = u % 7, bi = (14 * (n - N2) + u) % 3;
        if constexpr (u == 12) {
          WAITLGKM0();
          if constexpr (n + 1 == B_FLAG1) WAITVM(0);
          BAR();
          if constexpr (n + 1 == B_FLAG1) { if (cw == 0 && lane == 0) st_flag(ws, myflag1, tag); }
        }
        constexpr bool nxt = n + 1 < N3;
        if constexpr (u + 2 < 14) rdW(N, IC<u + 2>{});
        else if constexpr (nxt) rdW(IC<n + 1>{}, IC<u + 2 - 14>{});
        if constexpr (u == 2) rdT(N, IC<1>{});
        if constexpr (u == 12 && nxt) rdT(IC<n + 1>{}, IC<0>{});
#pragma unroll
        for (int a = 0; a < 2; ++a) {
          hg[a][m] = mfma(wg[bi], t[kap][a], hg[a][m]);
          hu[a][m] = mfma(wu[bi], t[kap][a], hu[a][m]);
        }
        constexpr int nr = ((u + 2 < 14 || nxt) ? 4 : 0) + ((u == 2 || (u == 12 && nxt)) ? 2 : 0);
        if constexpr (nr > 0) interleave<nr, 4>();
        SBAR();
      });
    });
  }
  float gs[2];
  {
    const unsigned hb = WS_H + (I * 4 + q) * 114688u + (2 * cw) * 1024u + late(lane) * 16u;
    const unsigned hp = hb + ((7 * j + 1) >> 1) * 8192u;
    const unsigned hs = hb + ((j & 1) ? ((7 * j) >> 1) * 8192u + 8u : (((7 * j) >> 1) + 3) * 8192u);
#pragma unroll
    for (int a = 0; a < 2; ++a) {
      const float L2E = 1.44269504088896341f;
      gs[a] = __builtin_amdgcn_rcpf(1.f + __builtin_amdgcn_exp2f(-L2E * lg[a]));
      const float inv = fminf(1.f + __builtin_amdgcn_exp2f(L2E * lg[a]), 1e30f);
      unsigned h2[7][2];
#pragma unroll
      for (int m = 0; m < 7; ++m) {
        float h[4];
#pragma unroll
        for (int r = 0; r < 4; ++r) {
          const float e = __builtin_amdgcn_exp2f(-L2E * hg[a][m][r]);
          h[r] = hu[a][m][r] * __builtin_amdgcn_rcpf(__builtin_fmaf(e, inv, inv));
        }
        h2[m][0] = pk2(h[0], h[1]);
        h2[m][1] = pk2(h[2], h[3]);
      }
#pragma unroll
      for (int v = 0; v < 3; ++v) {
        const u32x4 d = {h2[2 * v][0], h2[2 * v][1], h2[2 * v + 1][0], h2[2 * v + 1][1]};
        __builtin_amdgcn_raw_buffer_store_b128(d, rWs, hp + v * 8192 + a * 1024, 0, AUX_SC1);
      }
      const u32x2 d2 = {h2[6][0], h2[6][1]};
      __builtin_amdgcn_raw_buffer_store_b64(d2, rWs, hs + a * 1024, 0, AUX_SC1);
    }
  }

  f32x4 acc[2][7];
#pragma unroll
  for (int a = 0; a < 2; ++a)
#pragma unroll
    for (int m = 0; m < 7; ++m) acc[a][m] = f32x4{0, 0, 0, 0};
  {
    const int l3 = late(lane), g = l3 >> 4, c = l3 & 15;
    const int oT0 = (32 * cw + c) * 128 + ((g ^ (c >> 1)) << 4), oT1 = oT0 ^ 64;
    const int oE = 16384 + l3 * 16;
    const int oH = 2 * cw * 1024 + l3 * 16;
    const int oD = 16384 + (4 * g + (c >> 2)) * 224 + 8 * (c & 3);
    const unsigned myflag2 = WS_FLAG2 + ((I * 4 + q) * 4 + j) * 8u;
    bf16x8 ft[2][2], fe[2][7];
    auto rd = [&](auto N, auto H) {
      constexpr int n = N.value, h = H.value;
      LAS char* slot = lds + st_off<n>();
      if constexpr (n < N4) {
#pragma unroll
        for (int a = 0; a < 2; ++a) ft[h][a] = ldsr(slot + (h ? oT1 : oT0) + a * 2048);
#pragma unroll
        for (int m = 0; m < 7; ++m) fe[h][m] = ldsr(slot + oE + h * 7168 + m * 1024);
      } else {
#pragma unroll
        for (int a = 0; a < 2; ++a) ft[h][a] = ldsr(slot + oH + h * 8192 + a * 1024);
#pragma unroll
        for (int m = 0; m < 7; ++m) {
          LAS char* p = slot + oD + h * 7168 + m * 32;
          fe[h][m] = ldstr(p, p + 3584);
        }
      }
    };
    auto mm = [&](auto H) {
      constexpr int h = H.value;
#pragma unroll
      for (int m = 0; m < 7; ++m)
#pragma unroll
        for (int a = 0; a < 2; ++a) acc[a][m] = mfma(fe[h][m], ft[h][a], acc[a][m]);
    };
    rd(IC<N3>{}, IC<0>{});
    static_for<N3, NST>([&](auto N) {
      constexpr int n = N.value;
      rd(N, IC<1>{});
      mm(IC<0>{});
      interleave<(n < N4 ? 9 : 16), 14>();
      SBAR();
      if constexpr (n + 1 < NST) {
        WAITLGKM0();
        if constexpr (n + 1 == B_FLAG2) WAITVM(0);
        BAR();
        if constexpr (n + 1 == B_FLAG2) { if (cw == 0 && lane == 0) st_flag(ws, myflag2, tag); }
        rd(IC<n + 1>{}, IC<0>{});
      }
      mm(IC<1>{});
      if constexpr (n + 1 < NST) interleave<(n + 1 < N4 ? 9 : 16), 14>();
      SBAR();
      if constexpr (n + 1 == N4) {
#pragma unroll
        for (int a = 0; a < 2; ++a)
#pragma unroll
          for (int m = 0; m < 7; ++m) acc[a][m] *= gs[a];
        SBAR();
      }
    });
  }

  const int l4 = late(lane), g = l4 >> 4, c = l4 & 15;
  const unsigned inb = WS_INBOX + b * 32768u + cw * 2048u + l4 * 16u;
  f32x4 own[4];
#pragma unroll
  for (int k = 0; k < 4; ++k) own[k] = f32x4{0, 0, 0, 0};
  static_for<0, 4>([&](auto Q) {
    constexpr int qd = Q.value, a = qd & 1, mb = (qd >> 1) * 4;
    if (qd != q) {
      unsigned d[8];
#pragma unroll
      for (int k = 0; k < 4; ++k) {
        if (mb + k < 7) {
          const f32x4 x = acc[a][mb + k < 7 ? mb + k : 0];
          const unsigned u0 = pk2(x[0], x[1]), u1 = pk2(x[2], x[3]);
          d[2 * k] = u0 < SENT ? u0 : SENT - 1;
          d[2 * k + 1] = u1 < SENT ? u1 : SENT - 1;
        } else { d[2 * k] = 0; d[2 * k + 1] = 0; }
      }
      const unsigned o = WS_INBOX + ((b & ~3) | qd) * 32768u + q * 8192u + cw * 2048u + l4 * 16u;
      __builtin_amdgcn_raw_buffer_store_b128(u32x4{d[0], d[1], d[2], d[3]}, rWs, o, 0, AUX_SC1);
      __builtin_amdgcn_raw_buffer_store_b128(u32x4{d[4], d[5], d[6], d[7]}, rWs, o + 1024, 0, AUX_SC1);
    } else {
#pragma unroll
      for (int k = 0; k < 4; ++k) if (mb + k < 7) own[k] = acc[a][mb + k < 7 ? mb + k : 0];
    }
  });
  u32x4 in[6], sa[6], sb[6];
  const int mb = (q >> 1) * 4;
  unsigned ob = (128 * I + 32 * cw + 16 * (q & 1) + c) * 896u + (112 * j + 16 * mb + 4 * g) * 2u;
  {
    const unsigned o0 = inb + ((q + 1) & 3) * 8192u, o1 = inb + ((q + 2) & 3) * 8192u, o2 = inb + ((q + 3) & 3) * 8192u;
    unsigned left;
    asm volatile("" : "+v"(ob));
    asm volatile(
      "s_nop 4\n\t"
      "s_mov_b32 %[left], %[nmax]\n\t"
      "buffer_load_dwordx4 v[128:131], %[o0], %[rs], 0 offen sc1\n\t"
      "buffer_load_dwordx4 v[132:135], %[o0], %[rs], 0 offen offset:1024 sc1\n\t"
      "buffer_load_dwordx4 v[136:139], %[o1], %[rs], 0 offen sc1\n\t"
      "buffer_load_dwordx4 v[140:143], %[o1], %[rs], 0 offen offset:1024 sc1\n\t"
      "buffer_load_dwordx4 v[144:147], %[o2], %[rs], 0 offen sc1\n\t"
      "buffer_load_dwordx4 v[148:151], %[o2], %[rs], 0 offen offset:1024 sc1\n\t"
      "s_sleep 16\n"
      ".Lfk_poll:\n\t"
      "buffer_load_dwordx4 v[152:155], %[o0], %[rs], 0 offen sc1\n\t"
      "buffer_load_dwordx4 v[156:159], %[o0], %[rs], 0 offen offset:1024 sc1\n\t"
      "buffer_load_dwordx4 v[160:163], %[o1], %[rs], 0 offen sc1\n\t"
      "buffer_load_dwordx4 v[164:167], %[o1], %[rs], 0 offen offset:1024 sc1\n\t"
      "buffer_load_dwordx4 v[168:171], %[o2], %[rs], 0 offen sc1\n\t"
      "buffer_load_dwordx4 v[172:175], %[o2], %[rs], 0 offen offset:1024 sc1\n\t"
      "s_waitcnt vmcnt(6)\n\t"
      "v_max3_u32 v200, v128, v129, v130\n\t"
      "v_max3_u32 v200, v200, v131, v132\n\t"
      "v_max3_u32 v200, v200, v133, v134\n\t"
      "v_max3_u32 v200, v200, v135, v136\n\t"
      "v_max3_u32 v200, v200, v137, v138\n\t"
      "v_max3_u32 v200, v200, v139, v140\n\t"
      "v_max3_u32 v200, v200, v141, v142\n\t"
      "v_max3_u32 v200, v200, v143, v144\n\t"
      "v_max3_u32 v200, v200, v145, v146\n\t"
      "v_max3_u32 v200, v200, v147, v148\n\t"
      "v_max3_u32 v200, v200, v149, v150\n\t"
      "v_max_u32 v200, v200, v151\n\t"
      "v_cmp_eq_u32 vcc, -1, v200\n\t"
      "s_cmp_eq_u64 vcc, 0\n\t"
      "s_cbranch_scc1 .Lfk_poll_a\n\t"
      "s_sub_u32 %[left], %[left], 1\n\t"
      "s_cbranch_scc1 .Lfk_poll_a\n\t"
      "buffer_load_dwordx4 v[128:131], %[o0], %[rs], 0 offen sc1\n\t"
      "buffer_load_dwordx4 v[132:135], %[o0], %[rs], 0 offen offset:1024 sc1\n\t"
      "buffer_load_dwordx4 v[136:139], %[o1], %[rs], 0 offen sc1\n\t"
      "buffer_load_dwordx4 v[140:143], %[o1], %[rs], 0 offen offset:1024 sc1\n\t"
      "buffer_load_dwordx4 v[144:147], %[o2], %[rs], 0 offen sc1\n\t"
      "buffer_load_dwordx4 v[148:151], %[o2], %[rs], 0 offen offset:1024 sc1\n\t"
      "s_waitcnt vmcnt(6)\n\t"
      "v_max3_u32 v200, v152, v153, v154\n\t"
      "v_max3_u32 v200, v200, v155, v156\n\t"
      "v_max3_u32 v200, v200, v157, v158\n\t"
      "v_max3_u32 v200, v200, v159, v160\n\t"
      "v_max3_u32 v200, v200, v161, v162\n\t"
      "v_max3_u32 v200, v200, v163, v164\n\t"
      "v_max3_u32 v200, v200, v165, v166\n\t"
      "v_max3_u32 v200, v200, v167, v168\n\t"
      "v_max3_u32 v200, v200, v169, v170\n\t"
      "v_max3_u32 v200, v200, v171, v172\n\t"
      "v_max3_u32 v200, v200, v173, v174\n\t"
      "v_max_u32 v200, v200, v175\n\t"
      "v_cmp_eq_u32 vcc, -1, v200\n\t"
      "s_cmp_eq_u64 vcc, 0\n\t"
      "s_cbranch_scc0 .Lfk_poll\n\t"
      "v_mov_b64 v[176:177], v[152:153]\n\t"
      "v_mov_b64 v[178:179], v[154:155]\n\t"
      "v_mov_b64 v[180:181], v[156:157]\n\t"
      "v_mov_b64 v[182:183], v[158:159]\n\t"
      "v_mov_b64 v[184:185], v[160:161]\n\t"
      "v_mov_b64 v[186:187], v[162:163]\n\t"
      "v_mov_b64 v[188:189], v[164:165]\n\t"
      "v_mov_b64 v[190:191], v[166:167]\n\t"
      "v_mov_b64 v[192:193], v[168:169]\n\t"
      "v_mov_b64 v[194:195], v[170:171]\n\t"
      "v_mov_b64 v[196:197], v[172:173]\n\t"
      "v_mov_b64 v[198:199], v[174:175]\n\t"
      "s_branch .Lfk_poll_end\n"
      ".Lfk_poll_a:\n\t"
      "v_mov_b64 v[176:177], v[128:129]\n\t"
      "v_mov_b64 v[178:179], v[130:131]\n\t"
      "v_mov_b64 v[180:181], v[132:133]\n\t"
      "v_mov_b64 v[182:183], v[134:135]\n\t"
      "v_mov_b64 v[184:185], v[136:137]\n\t"
      "v_mov_b64 v[186:187], v[138:139]\n\t"
      "v_mov_b64 v[188:189], v[140:141]\n\t"
      "v_mov_b64 v[190:191], v[142:143]\n\t"
      "v_mov_b64 v[192:193], v[144:145]\n\t"
      "v_mov_b64 v[194:195], v[146:147]\n\t"
      "v_mov_b64 v[196:197], v[148:149]\n\t"
      "v_mov_b64 v[198:199], v[150:151]\n\t"
      ".Lfk_poll_end:"
      : "=&{v[176:179]}"(in[0]), "=&{v[180:183]}"(in[1]), "=&{v[184:187]}"(in[2]), "=&{v[188:191]}"(in[3]), "=&{v[192:195]}"(in[4]), "=&{v[196:199]}"(in[5]),
        "=&{v[128:131]}"(sa[0]), "=&{v[132:135]}"(sa[1]), "=&{v[136:139]}"(sa[2]), "=&{v[140:143]}"(sa[3]), "=&{v[144:147]}"(sa[4]), "=&{v[148:151]}"(sa[5]),
        "=&{v[152:155]}"(sb[0]), "=&{v[156:159]}"(sb[1]), "=&{v[160:163]}"(sb[2]), "=&{v[164:167]}"(sb[3]), "=&{v[168:171]}"(sb[4]), "=&{v[172:175]}"(sb[5]),
        [left] "=&s"(left)
      : [o0] "v"(o0), [o1] "v"(o1), [o2] "v"(o2), [rs] "s"(rWs), [nmax] "n"(SPIN_MAX)
      : "v200", "vcc", "scc", "memory");
  }
  {
    const rsrc_t rO = mkrs(out, 2048u * 896u);
#pragma unroll
    for (int k = 0; k < 4; ++k) {
      f32x4 s = own[k];
#pragma unroll
      for (int d = 0; d < 3; ++d) {
        const unsigned u0 = in[2 * d + (k >> 1)][2 * (k & 1)], u1 = in[2 * d + (k >> 1)][2 * (k & 1) + 1];
        s += f32x4{bflo(u0), bfhi(u0), bflo(u1), bfhi(u1)};
      }
      if (k < 3 || mb == 0) {
        const u32x2 o2 = {pk2(s[0], s[1]), pk2(s[2], s[3])};
        __builtin_amdgcn_raw_buffer_store_b64(o2, rO, ob + 32 * k, 0, 0);
      }
    }
  }
  asm volatile("s_waitcnt vmcnt(3)"
      :: "{v[128:131]}"(sa[0]), "{v[132:135]}"(sa[1]), "{v[136:139]}"(sa[2]), "{v[140:143]}"(sa[3]), "{v[144:147]}"(sa[4]), "{v[148:151]}"(sa[5]),
         "{v[152:155]}"(sb[0]), "{v[156:159]}"(sb[1]), "{v[160:163]}"(sb[2]), "{v[164:167]}"(sb[3]), "{v[168:171]}"(sb[4]), "{v[172:175]}"(sb[5])
      : "memory");
}

__global__ __launch_bounds__(512, 2) void fused_kernel(const void* Ax, const void* Cre, const void* Cim, const void* Wdre, const void* Wdim,
                                                       const void* Wg, const void* Wu, const void* Wdn, const void* gw, void* out, char* ws) {
  __shared__ __attribute__((aligned(1024))) char shm[LDS_BYTES];
  LAS char* lds = (LAS char*)shm;
  {
    SBAR();
    u64 pc;
    unsigned len, o0, o1, oc;
    asm volatile("s_getpc_b64 %0\n.Lfk_entry:\n\ts_mov_b32 %1, .Lfk_code_end-.Lfk_entry\n\ts_mov_b32 %2, .Lfk_loader0-.Lfk_entry-256\n\t"
                 "s_mov_b32 %3, .Lfk_loader1-.Lfk_entry-256\n\ts_mov_b32 %4, .Lfk_loaders-.Lfk_entry"
                 : "=s"(pc), "=s"(len), "=s"(o0), "=s"(o1), "=s"(oc));
    const unsigned w = threadIdx.x >> 6, l = threadIdx.x & 63;
    const unsigned o = w < 2 && l < 36 ? (l < 4 ? oc : w ? o1 : o0) + l * 64u : 0x80000000u;
    __builtin_amdgcn_raw_ptr_buffer_load_lds(mkrs((const void*)pc, len), (LAS void*)(lds + PF_OFF), 4, o, 0, 0, 0);
    SBAR();
  }
  asm volatile("" : "+s"(Ax), "+s"(Cre), "+s"(Cim), "+s"(Wdre), "+s"(Wdim), "+s"(Wg), "+s"(Wu), "+s"(Wdn), "+s"(gw), "+s"(out), "+s"(ws));
  Geo G;
  G.lane = threadIdx.x & 63;
  G.wid = __builtin_amdgcn_readfirstlane(threadIdx.x >> 6);
  G.b = blockIdx.x;
  G.q = G.b & 3;
  G.r = (G.b >> 2) & 1;
  G.j = (G.b >> 3) & 3;
  G.i = G.b >> 5;
  G.I = 8 * G.r + G.i;
  G.k7 = (G.i + 2 * G.j) % 7;
  G.k16 = 2 * G.i;
  if (G.wid >= 4) {
    asm volatile(".Lfk_loaders:");
    SBAR();
    const bool im = G.q >= 2;
    const void* Cp = im ? Cim : Cre;
    const void* Wdp = im ? Wdim : Wdre;
    if (G.wid < 6) loader<0>(G, lds, ws, Ax, Cp, Wdp, Wg, Wu, Wdn);
    else loader<1>(G, lds, ws, Ax, Cp, Wdp, Wg, Wu, Wdn);
  } else {
    compute(G, lds, ws, gw, out, Wg, Wu, Wdn, G.q >= 2 ? Cim : Cre);
  }
  asm volatile(".Lfk_code_end:");
}

}

extern "C" void kernel_launch(void* const* d_in, const int* in_sizes, int n_in, void* d_out, int out_size, void* d_ws,
                              size_t ws_size, hipStream_t stream) {
  (void)in_sizes; (void)n_in; (void)out_size; (void)ws_size;
  fk::fused_kernel<<<256, 512, 0, stream>>>(d_in[0], d_in[1], d_in[2], d_in[3], d_in[4], d_in[6], d_in[7], d_in[8], d_in[9],
                                            d_out, (char*)d_ws);
}
```

```cpp
#include <hip/hip_runtime.h>
#include <hip/hip_bf16.h>
#include <stdint.h>

#define LAS __attribute__((address_space(3)))
#define GAS __attribute__((address_space(1)))

namespace fk {

typedef __bf16 bf16x2 __attribute__((ext_vector_type(2)));
typedef __bf16 bf16x4 __attribute__((ext_vector_type(4)));
typedef __bf16 bf16x8 __attribute__((ext_vector_type(8)));
typedef float f32x2 __attribute__((ext_vector_type(2)));
typedef float f32x4 __attribute__((ext_vector_type(4)));
typedef unsigned u32x2 __attribute__((ext_vector_type(2)));
typedef unsigned u32x4 __attribute__((ext_vector_type(4)));
typedef unsigned long long u64;
typedef __amdgpu_buffer_rsrc_t rsrc_t;

template <int V> struct IC { static constexpr int value = V; };
template <int B, int E, class Fn> __device__ __forceinline__ void static_for(Fn&& f) {
  if constexpr (B < E) { f(IC<B>{}); static_for<B + 1, E>(f); }
}

constexpr int NST = 37, N2 = 7, N3 = 14, N4 = 30;
constexpr int SZ1 = 22528, SZ2 = 45056, SZ3 = 30720;
constexpr int RING = 161792, GW_OFF = RING, PF_OFF = RING + 1024, LDS_BYTES = RING + 2048;
constexpr int st_size(int n) { return n < N2 ? SZ1 : (n < N3 ? SZ2 : SZ3); }
constexpr int st_szA(int n) { return n < N2 ? SZ1 : 16384; }
constexpr int cntA(int cls, int n) { return n < N2 ? (cls ? 5 : 6) : 4; }
constexpr int cntB(int cls, int n) { return n < N2 ? 0 : (n < N3 ? 7 : (cls ? 3 : 4)); }
constexpr int N_GAP = 19, T_GAP = 17;
struct Plan { int off[NST]; int atA[NST]; int atB[NST]; };
constexpr Plan make_plan() {
  Plan p{};
  constexpr int ova[10] = {-1, -1, 0, 0, 1, 2, 3, 4, 5, 7};
  constexpr int ovb[3] = {5, 6, 8};
  int cur = 0;
  for (int n = 0; n < NST; ++n) {
    if (cur + st_size(n) > RING) cur = 0;
    p.off[n] = cur;
    cur += st_size(n);
    int a = -1, b = -1;
    const int lo = p.off[n], mid = lo + st_szA(n), hi = lo + st_size(n);
    for (int m = 0; m < n; ++m) {
      const int ml = p.off[m], mh = ml + st_size(m);
      if (ml < mid && lo < mh) a = m + 1;
      if (mid < hi && ml < hi && mid < mh) b = m + 1;
    }
    if (n < 10 && a < ova[n]) a = ova[n];
    if (n >= N2 && n < 10 && b < ovb[n - N2]) b = ovb[n - N2];
    if (n >= 10) { if (a < b) a = b; b = a; }
    if (n > 0 && a < p.atA[n - 1]) a = p.atA[n - 1];
    if (n > N2 && b < p.atB[n - 1]) b = p.atB[n - 1];
    if (n == N_GAP && a < T_GAP) a = T_GAP;
    if ((n > N_GAP || (n > N3 && n < N3 + 3)) && a <= p.atA[n - 1]) a = p.atA[n - 1] + 1;
    if (n >= 10 && b < a) b = a;
    if (n >= 10 && a < b) a = b;
    p.atA[n] = a;
    p.atB[n] = n < N2 ? a : b;
  }
  return p;
}
constexpr Plan PLAN = make_plan();
template <int N> constexpr int st_off() { return PLAN.off[N]; }
template <int N> constexpr int st_atA() { return PLAN.atA[N]; }
template <int N> constexpr int st_atB() { return PLAN.atB[N]; }
constexpr int T_EPOCH = -1;
constexpr int T_PF1 = PLAN.atA[N3] - 2, T_PF2 = PLAN.atA[N4] - 2;
constexpr int T_C0 = 7, T_C1 = 9, T_C2 = 11, T_C3 = 13;
constexpr int T_D = 20;
constexpr int pf_cnt(int a) {
  return (a == T_C0) + (a == T_C1) + (a == T_C2) + (a == T_C3) + (a == T_C0 + 1) + (a == T_C1 + 1) + (a == T_C2 + 1) + (a == T_C3 + 1) +
         (a == T_D) + (a == T_EPOCH) + (a == T_PF1) + (a == T_PF2);
}
constexpr int st_wait_c(int cls, int n) {
  int c = 0;
  bool seenA = false, seenB = false;
  for (int t = -1; t <= n - 1; ++t) {
    for (int s = 0; s < NST; ++s) {
      if (PLAN.atA[s] == t) { if (s == n) { seenA = true; c = 0; } else c += cntA(cls, s); }
      if (PLAN.atB[s] == t && cntB(cls, s) > 0) { if (s == n) { seenB = true; c = 0; } else c += cntB(cls, s); }
    }
    c += pf_cnt(t);
  }
  return seenA && (seenB || cntB(cls, n) == 0) ? c : -1;
}
template <int CLS, int N> constexpr int st_wait() { return st_wait_c(CLS, N); }
constexpr bool plan_ok() {
  for (int n = 0; n < NST; ++n)
    for (int cls = 0; cls < 2; ++cls)
      if (st_wait_c(cls, n) < 0 || st_wait_c(cls, n) > 62) return false;
  return true;
}
static_assert(plan_ok(), "stage plan");

constexpr int B_TAG = 4;
constexpr int B_FLAG1 = 9;
constexpr int N_POLL1 = 11;
constexpr int B_FLAG2 = 21;

constexpr unsigned WS_EPOCH = 0;
constexpr unsigned WS_FLAG1 = 4096;
constexpr unsigned WS_FLAG2 = 8192;
constexpr unsigned WS_LOGIT = 16384;
constexpr unsigned WS_ABT = 131072;
constexpr unsigned WS_H = 4u << 20;
constexpr unsigned WS_INBOX = 12u << 20;
constexpr unsigned WS_END = 20u << 20;
constexpr unsigned SENT = 0xFFFFFFFFu;
constexpr int SPIN_MAX = 1 << 20;
constexpr int AUX_SC1 = 16;

#define BAR() do { asm volatile("" ::: "memory"); __builtin_amdgcn_s_barrier(); asm volatile("" ::: "memory"); } while (0)
#define WAITVM(n) asm volatile("s_waitcnt vmcnt(%0)" ::"n"(n) : "memory")
#define WAITLGKM0() do { __builtin_amdgcn_s_waitcnt(0xC07F); asm volatile("" ::: "memory"); } while (0)
#define SBAR() __builtin_amdgcn_sched_barrier(0)

__device__ __forceinline__ rsrc_t mkrs(const void* p, unsigned bytes) {
  return __builtin_amdgcn_make_buffer_rsrc((void*)p, 0, (int)bytes, 0x00020000);
}
template <int AUX> __device__ __forceinline__ void dma(rsrc_t rs, unsigned voff, unsigned soff, LAS char* l) {
  __builtin_amdgcn_raw_ptr_buffer_load_lds(rs, (LAS void*)l, 16, voff, soff, 0, AUX);
}
__device__ __forceinline__ bf16x8 ldsr(LAS char* p) { return *(LAS bf16x8*)p; }
__device__ __forceinline__ bf16x8 ldstr(LAS char* p0, LAS char* p1) {
  bf16x4 a = __builtin_amdgcn_ds_read_tr16_b64_v4bf16((LAS bf16x4*)p0);
  bf16x4 b = __builtin_amdgcn_ds_read_tr16_b64_v4bf16((LAS bf16x4*)p1);
  return __builtin_shufflevector(a, b, 0, 1, 2, 3, 4, 5, 6, 7);
}
__device__ __forceinline__ f32x4 mfma(bf16x8 a, bf16x8 b, f32x4 c) { return __builtin_amdgcn_mfma_f32_16x16x32_bf16(a, b, c, 0, 0, 0); }
__device__ __forceinline__ unsigned pk2(float x, float y) {
  f32x2 v = {x, y};
  return __builtin_bit_cast(unsigned, __builtin_convertvector(v, bf16x2));
}
__device__ __forceinline__ float bflo(unsigned d) { return __builtin_bit_cast(float, d << 16); }
__device__ __forceinline__ float bfhi(unsigned d) { return __builtin_bit_cast(float, d & 0xFFFF0000u); }
__device__ __forceinline__ u64 ld_flag(const char* ws, unsigned off) {
  return __hip_atomic_load((const GAS u64*)(ws + off), __ATOMIC_RELAXED, __HIP_MEMORY_SCOPE_AGENT);
}
__device__ __forceinline__ void st_flag(char* ws, unsigned off, u64 v) {
  __hip_atomic_store((GAS u64*)(ws + off), v, __ATOMIC_RELAXED, __HIP_MEMORY_SCOPE_AGENT);
}
__device__ __forceinline__ u64 make_tag(u64 epoch) {
  const unsigned lo = __builtin_amdgcn_readfirstlane((unsigned)epoch), hi = __builtin_amdgcn_readfirstlane((unsigned)(epoch >> 32));
  return (((u64)hi << 32) | lo) * 6364136223846793005ull + 1442695040888963407ull;
}
__device__ __forceinline__ void wait_flag(const char* ws, unsigned off, u64 v, u64 tag) {
  asm volatile("" : "+v"(v));
  if (__builtin_amdgcn_ballot_w64(v != tag) != 0) {
    for (int it = 0; it < SPIN_MAX; ++it) {
      __builtin_amdgcn_s_sleep(2);
      if (__builtin_amdgcn_ballot_w64(ld_flag(ws, off) != tag) == 0) break;
    }
  }
  asm volatile("" ::: "memory");
}
template <int NR, int NM> __device__ __forceinline__ void interleave() {
  static_for<0, NM>([&](auto K) {
    constexpr int k = K.value, cnt = NR / NM + (k < NR % NM ? 1 : 0);
    __builtin_amdgcn_sched_group_barrier(0x008, 1, 0);
    if constexpr (cnt > 0) __builtin_amdgcn_sched_group_barrier(0x100, cnt, 0);
  });
}

__device__ __forceinline__ int late(int x) { asm volatile("" : "+v"(x)); return x; }

struct Geo { int b, r, q, i, j, I, lane, wid, k7, k16; };
template <int NK> __device__ __forceinline__ unsigned kstep(const Geo& G, int k) {
  const unsigned x = (NK == 7 ? G.k7 : G.k16) + k;
  return x >= NK ? x - NK : x;
}

template <int CLS>
__device__ __forceinline__ void loader(const Geo& G, LAS char* lds, char* ws, const void* Ax, const void* Cp, const void* Wdp,
                                       const void* Wg, const void* Wu, const void* Wdn) {
  const int lane = G.lane, lw = G.wid - 4, q = G.q, j = G.j, I = G.I;
  asm volatile(".Lfk_loader%0:" ::"n"(CLS));
  SBAR();
  const rsrc_t rA = mkrs(Ax, 2048u * 896u), rWd = mkrs(Wdp, 448u * 896u);
  constexpr int NB = CLS ? 3 : 4;
  const int blk0 = CLS ? 8 + 3 * (lw - 2) : 4 * lw;
  unsigned vA[2], vWd[NB], vX[4], vW[7], vC[4], vWdn[NB];
  {
    const int r8 = lane >> 3, ch = lane & 7;
    const int sig = 2 * I + (q & 1);
#pragma unroll
    for (int n = 0; n < 2; ++n) {
      const int row = 16 * lw + 8 * n + r8;
      const int f = ((row >> 1) & 1) | (((row >> 3) & 3) << 1);
      vA[n] = (64 * sig + row) * 896u + ((ch ^ f) << 4);
    }
#pragma unroll
    for (int n = 0; n < NB; ++n) {
      const int row = 8 * (blk0 + n) + r8;
      vWd[n] = (112 * j + row) * 896u + ((ch ^ ((row >> 1) & 7)) << 4);
    }
  }
  const rsrc_t rC = mkrs(Cp, 2048u * 4096u), rWs = mkrs(ws, WS_END), rWdn = mkrs(Wdn, 1792u * 896u);
  const rsrc_t rW = mkrs(lw < 2 ? Wg : Wu, 448u * 3584u);
  unsigned vAB, vH, fo1, fo2, tD, tC, tK;
  u64 tag = 0, f1 = 0, f2 = 0;

  auto issueA = [&](auto S) {
    constexpr int s = S.value;
    LAS char* slot = lds + st_off<s>();
    if constexpr (s < N2) {
#pragma unroll
      for (int n = 0; n < 2; ++n) dma<0>(rA, vA[n], 128 * kstep<7>(G, s), slot + (2 * lw + n) * 1024);
#pragma unroll
      for (int n = 0; n < NB; ++n) dma<0>(rWd, vWd[n], 128 * kstep<7>(G, s), slot + 8192 + (blk0 + n) * 1024);
    } else if constexpr (s < N3) {
      const unsigned k = kstep<7>(G, s - N2);
#pragma unroll
      for (int n = 0; n < 4; ++n) dma<0>(rA, vX[n], 128 * k, slot + (4 * lw + n) * 1024);
    } else if constexpr (s < N4) {
      const unsigned k = kstep<16>(G, s - N3);
      if constexpr (s == N3) wait_flag(ws, fo1, f1, tag);
#pragma unroll
      for (int n = 0; n < 4; ++n) dma<0>(rC, vC[n], 128 * k, slot + (4 * lw + n) * 1024);
    } else {
      const unsigned k = kstep<7>(G, s - N4);
      if constexpr (s == N4) wait_flag(ws, fo2, f2, tag);
#pragma unroll
      for (int n = 0; n < 4; ++n) dma<AUX_SC1>(rWs, vH, 16384 * k + 1024 * n, slot + (4 * lw + n) * 1024);
    }
  };
  auto issueB = [&](auto S) {
    constexpr int s = S.value;
    LAS char* slot = lds + st_off<s>();
    if constexpr (s >= N2 && s < N3) {
      const unsigned k = kstep<7>(G, s - N2);
#pragma unroll
      for (int n = 0; n < 7; ++n) dma<0>(rW, vW[n], 229376 * k, slot + 16384 + (7 * lw + n) * 1024);
    } else if constexpr (s >= N3 && s < N4) {
      const unsigned k = kstep<16>(G, s - N3);
#pragma unroll
      for (int n = 0; n < NB; ++n) dma<AUX_SC1>(rWs, vAB, 14336 * k + 1024 * n, slot + 16384 + (blk0 + n) * 1024);
    } else if constexpr (s >= N4) {
      const unsigned k = kstep<7>(G, s - N4);
#pragma unroll
      for (int n = 0; n < NB; ++n) dma<0>(rWdn, vWdn[n], 57344 * k, slot + 16384 + (blk0 + n) * 1024);
    }
  };
  auto issue_at = [&](auto T) {
    static_for<0, NST>([&](auto S) {
      if constexpr (st_atA<S.value>() == T.value) issueA(S);
      if constexpr (st_atB<S.value>() == T.value) issueB(S);
    });
  };
  auto touch = [&](rsrc_t rs, unsigned voff, unsigned soff) {
    __builtin_amdgcn_raw_ptr_buffer_load_lds(rs, (LAS void*)(lds + PF_OFF), 4, voff, soff, 0, 0);
  };
  auto vpf = [&](auto A) {
    constexpr int a = A.value;
    auto touchC = [&](int e, int par) {
      const unsigned o = tC + (((tK + 4 * e) & 15) << 7);
      touch(rC, (G.i & 1) == par ? o : 0x80000000u, 0);
    };
    if constexpr (a == T_C0) touchC(0, 0);
    if constexpr (a == T_C1) touchC(1, 0);
    if constexpr (a == T_C2) touchC(2, 0);
    if constexpr (a == T_C3) touchC(3, 0);
    if constexpr (a == T_C0 + 1) touchC(0, 1);
    if constexpr (a == T_C1 + 1) touchC(1, 1);
    if constexpr (a == T_C2 + 1) touchC(2, 1);
    if constexpr (a == T_C3 + 1) touchC(3, 1);
    if constexpr (a == T_D) touch(rWdn, tD, 0);
    if constexpr (a == T_PF1) f1 = ld_flag(ws, fo1);
    if constexpr (a == T_PF2) f2 = ld_flag(ws, fo2);
  };
  issue_at(IC<-1>{});
  const int ln = late(lane);
  u64 epoch = ld_flag(ws, WS_EPOCH);
  {
    const int r8 = ln >> 3, ch = ln & 7;
#pragma unroll
    for (int n = 0; n < 4; ++n) {
      const int row = 32 * lw + 8 * n + r8;
      const unsigned sw = (ch ^ ((row >> 1) & 7)) << 4;
      vX[n] = (128 * I + row) * 896u + sw;
      vC[n] = (128 * I + row) * 4096u + sw + 2048u * (q & 1);
    }
#pragma unroll
    for (int n = 0; n < 7; ++n) {
      const int idx = 7 * lw + n, ii = idx >= 14 ? idx - 14 : idx;
      const unsigned c16 = ii * 64 + ln, pr = (c16 * 4682u) >> 16, cc = c16 - 14u * pr;
      const unsigned rho = (pr & ~12u) | ((pr & 8) >> 1) | ((pr & 4) << 1);
      vW[n] = rho * 3584u + cc * 16u + (448u * q + 112u * j) * 2u;
    }
#pragma unroll
    for (int n = 0; n < NB; ++n) {
      const unsigned c16 = (blk0 + n) * 64 + ln, pr = (c16 * 4682u) >> 16, cc = c16 - 14u * pr;
      vWdn[n] = (448 * q + pr) * 896u + 224u * j + cc * 16u;
    }
    vAB = WS_ABT + (q * 4 + j) * 16u * 14336u + blk0 * 1024u + ln * 16u;
    vH = WS_H + (I * 4 + q) * 114688u + lw * 4096u + ln * 16u;
    fo1 = WS_FLAG1 + ((q * 4 + j) * 16 + (ln & 15)) * 8u;
    fo2 = WS_FLAG2 + ((I * 4 + q) * 4 + (ln & 3)) * 8u;
    const unsigned pid = ((G.i * 4 + j) * 4 + lw) * 49 + ln;
    tD = ln < 49 ? 448u * 896u * q + pid * 64u : 0x80000000u;
    tC = (128 * I + 32 * j + 8 * lw + ((ln >> 1) & 7)) * 4096u + 2048u * (q & 1) + (ln & 1) * 64u;
    tK = kstep<16>(G, 0) + (ln >> 4);
  }
  vpf(IC<-1>{});
  static_for<0, NST>([&](auto N) {
    constexpr int n = N.value;
    WAITVM((st_wait<CLS, n>()));
    BAR();
    if constexpr (n == B_TAG) {
      asm volatile("" : "+v"(epoch));
      tag = make_tag(epoch);
    }
    issue_at(N);
    vpf(N);
  });
  if (G.b == 0 && lw == 3) {
#pragma unroll
    for (int k = 0; k < 4; ++k) {
      const unsigned fo = WS_FLAG1 + (k * 64 + lane) * 8u;
      wait_flag(ws, fo, ld_flag(ws, fo), tag);
    }
    if (lane == 0) st_flag(ws, WS_EPOCH, tag);
  }
}

__device__ __forceinline__ void clear_inbox(const Geo& G, rsrc_t rWs) {
#pragma unroll
  for (int d = 1; d < 4; ++d) {
    const u32x4 s4 = {SENT, SENT, SENT, SENT};
    const unsigned o = WS_INBOX + G.b * 32768u + G.wid * 2048u + late(G.lane) * 16u + ((G.q + d) & 3) * 8192u;
    __builtin_amdgcn_raw_buffer_store_b128(s4, rWs, o, 0, AUX_SC1);
    __builtin_amdgcn_raw_buffer_store_b128(s4, rWs, o + 1024, 0, AUX_SC1);
  }
}
template <int EH>
__device__ __forceinline__ void g1_phase(const Geo& G, LAS char* lds, char* ws, rsrc_t rWs) {
  const int lane = G.lane, cw = G.wid, g = lane >> 4, c = lane & 15, sh = cw >> 1;
  f32x4 acc[2][4];
#pragma unroll
  for (int a = 0; a < 2; ++a)
#pragma unroll
    for (int k = 0; k < 4; ++k) acc[a][k] = f32x4{0, 0, 0, 0};
  const int fA = ((c >> 1) & 1) | ((c >> 2) << 1);
  const int oA0 = (32 * sh + 8 * (c >> 2) + (c & 3)) * 128 + ((g ^ fA) << 4), oA1 = oA0 ^ 64;
  const int oB0 = 8192 + (64 * EH + c) * 128 + ((g ^ (c >> 1)) << 4), oB1 = oB0 ^ 64;
  const int oG = GW_OFF + 16 * g;
  bf16x8 fa[2][2], fb[2][4];
  auto rd = [&](auto N, auto H) {
    constexpr int n = N.value, h = H.value;
    LAS char* slot = lds + st_off<n>();
#pragma unroll
    for (int a = 0; a < 2; ++a) fa[h][a] = ldsr(slot + (h ? oA1 : oA0) + a * 512);
#pragma unroll
    for (int k = 0; k < 4; ++k) {
      if (EH && k == 3) fb[h][k] = ldsr(lds + (oG + 128 * kstep<7>(G, n)) + 64 * h);
      else fb[h][k] = ldsr(slot + (h ? oB1 : oB0) + k * 2048);
    }
  };
  auto mm = [&](auto H) {
    constexpr int h = H.value;
#pragma unroll
    for (int k = 0; k < 4; ++k)
#pragma unroll
      for (int a = 0; a < 2; ++a) acc[a][k] = mfma(fa[h][a], fb[h][k], acc[a][k]);
  };
  BAR();
  rd(IC<0>{}, IC<0>{});
  static_for<0, N2>([&](auto N) {
    constexpr int n = N.value;
    rd(N, IC<1>{});
    mm(IC<0>{});
    interleave<6, 8>();
    SBAR();
    WAITLGKM0();
    BAR();
    if constexpr (n == 0) clear_inbox(G, rWs);
    if constexpr (n + 1 < N2) rd(IC<n + 1>{}, IC<0>{});
    mm(IC<1>{});
    if constexpr (n + 1 < N2) interleave<6, 8>();
    SBAR();
  });
  const int p = G.q >> 1;
  const unsigned base = WS_ABT + (((2 * p + G.r) * 4 + G.j) * 16 + 2 * G.i + (G.q & 1)) * 14336u + sh * 7168u + EH * 4096u + lane * 16u;
  const float sg = p ? -1.f : 1.f;
#pragma unroll
  for (int k = 0; k < (EH ? 3 : 4); ++k) {
    const f32x4 x = acc[0][k] * sg, y = acc[1][k] * sg;
    const u32x4 v = {pk2(x[0], x[1]), pk2(x[2], x[3]), pk2(y[0], y[1]), pk2(y[2], y[3])};
    __builtin_amdgcn_raw_buffer_store_b128(v, rWs, base + k * 1024, 0, AUX_SC1);
  }
  if (EH && c == 0) {
    const unsigned lo = WS_LOGIT + G.b * 256u + (32 * sh + 8 * g) * 4u;
    __builtin_amdgcn_raw_buffer_store_b128(__builtin_bit_cast(u32x4, acc[0][3]), rWs, lo, 0, AUX_SC1);
    __builtin_amdgcn_raw_buffer_store_b128(__builtin_bit_cast(u32x4, acc[1][3]), rWs, lo + 16, 0, AUX_SC1);
  }
}

__device__ __forceinline__ void compute(const Geo& G, LAS char* lds, char* ws, const void* gw, void* out, const void* Wg,
                                        const void* Wu, const void* Wdn, const void* Cp) {
  const int lane = G.lane, cw = G.wid, q = G.q, j = G.j, I = G.I, b = G.b;
  const u64 epoch = ld_flag(ws, WS_EPOCH);
  const rsrc_t rWs = mkrs(ws, WS_END);
  {
    u64 pc;
    unsigned len;
    asm volatile("s_getpc_b64 %0\n.Lfk_code_begin:\n\ts_mov_b32 %1, .Lfk_code_end-.Lfk_code_begin" : "=s"(pc), "=s"(len));
    const unsigned o = lane < 7 ? (((b >> 3) * 4 + cw) * 7 + lane) * 64u : 0x80000000u;
    __builtin_amdgcn_raw_ptr_buffer_load_lds(mkrs((const void*)pc, len), (LAS void*)(lds + PF_OFF), 4, o, 0, 0, 0);
  }
  if (cw == 2) {
    const unsigned last = lane & 1;
    const char* pa = (const char*)Wg + last * (448u * 3584u - 4);
    if ((lane >> 1) == 1) pa = (const char*)Wu + last * (448u * 3584u - 4);
    if ((lane >> 1) == 2) pa = (const char*)Wdn + 401408u * q + last * 401404u;
    if ((lane >> 1) == 3) pa = (const char*)Cp + 524288u * I + last * 524284u;
    if (lane < 8) __builtin_amdgcn_global_load_lds((const GAS void*)pa, (LAS void*)(lds + PF_OFF + 512), 4, 0, 0);
  }
  if (cw == 1) {
    const rsrc_t rG = mkrs(gw, 896u);
    const u32x4 v = __builtin_amdgcn_raw_buffer_load_b128(rG, lane * 16u, 0, 0);
    *(LAS u32x4*)(lds + GW_OFF + lane * 16) = v;
    WAITLGKM0();
  }
  __builtin_amdgcn_s_waitcnt(0x0F70);
  const u64 tag = make_tag(epoch);
  if (cw & 1) g1_phase<1>(G, lds, ws, rWs);
  else g1_phase<0>(G, lds, ws, rWs);

  const unsigned myflag1 = WS_FLAG1 + (((2 * (q >> 1) + G.r) * 4 + j) * 16 + 2 * G.i + (q & 1)) * 8u;
  float lg[2];
  f32x4 hg[2][7], hu[2][7];
#pragma unroll
  for (int a = 0; a < 2; ++a)
#pragma unroll
    for (int m = 0; m < 7; ++m) { hg[a][m] = f32x4{0, 0, 0, 0}; hu[a][m] = f32x4{0, 0, 0, 0}; }
  {
    const int l2 = late(lane), g = l2 >> 4, c = l2 & 15;
    const int oT0 = (32 * cw + c) * 128 + ((g ^ (c >> 1)) << 4), oT1 = oT0 ^ 64;
    const int oWb = 16384 + (16 * (g >> 1) + 4 * (g & 1) + (c >> 2)) * 224 + 8 * (c & 3);
    const int oWP = oWb + 32 * (j & 1), oWS = oWb + ((j & 1) ? 0 : 192);
    bf16x8 t[2][2], wg[3], wu[3];
    u64 fv = 0;
    auto rdW = [&](auto N, auto U) {
      constexpr int n = N.value, u = U.value, kap = u / 7, m = u % 7, bi = (14 * (n - N2) + u) % 3;
      LAS char* p = lds + st_off<n>() + (m < 6 ? oWP + 32 * m : oWS) + kap * 7168;
      wg[bi] = ldstr(p, p + 1792);
      wu[bi] = ldstr(p + 14336, p + 14336 + 1792);
    };
    auto rdT = [&](auto N, auto H) {
      constexpr int n = N.value, h = H.value;
#pragma unroll
      for (int a = 0; a < 2; ++a) t[h][a] = ldsr(lds + st_off<n>() + (h ? oT1 : oT0) + a * 2048);
    };
    rdT(IC<N2>{}, IC<0>{});
    rdW(IC<N2>{}, IC<0>{});
    rdW(IC<N2>{}, IC<1>{});
    SBAR();
    static_for<N2, N3>([&](auto N) {
      constexpr int n = N.value;
      if constexpr (n == N_POLL1 || n == N_POLL1 + 1) {
        const int lp = late(lane), qq = lp & 3;
        const unsigned fo4 = WS_FLAG1 + (((2 * (qq >> 1) + G.r) * 4 + j) * 16 + 2 * G.i + (qq & 1)) * 8u;
        if constexpr (n == N_POLL1) fv = ld_flag(ws, fo4);
        else wait_flag(ws, fo4, fv, tag);
      }
      if constexpr (n == N_POLL1 + 1) {
        const unsigned lgo = WS_LOGIT + ((b & ~1) | (cw >> 1)) * 256u + (32 * (cw & 1) + (late(lane) & 15)) * 4u;
#pragma unroll
        for (int a = 0; a < 2; ++a) lg[a] = __builtin_bit_cast(float, __builtin_amdgcn_raw_buffer_load_b32(rWs, lgo + 64 * a, 0, AUX_SC1));
      }
      static_for<0, 14>([&](auto U) {
        constexpr int u = U.value, kap = u / 7, m = u % 7, bi = (14 * (n - N2) + u) % 3;
        if constexpr (u == 12) {
          WAITLGKM0();
          if constexpr (n + 1 == B_FLAG1) WAITVM(0);
          BAR();
          if constexpr (n + 1 == B_FLAG1) { if (cw == 0 && lane == 0) st_flag(ws, myflag1, tag); }
        }
        constexpr bool nxt = n + 1 < N3;
        if constexpr (u + 2 < 14) rdW(N, IC<u + 2>{});
        else if constexpr (nxt) rdW(IC<n + 1>{}, IC<u + 2 - 14>{});
        if constexpr (u == 2) rdT(N, IC<1>{});
        if constexpr (u == 12 && nxt) rdT(IC<n + 1>{}, IC<0>{});
#pragma unroll
        for (int a = 0; a < 2; ++a) {
          hg[a][m] = mfma(wg[bi], t[kap][a], hg[a][m]);
          hu[a][m] = mfma(wu[bi], t[kap][a], hu[a][m]);
        }
        constexpr int nr = ((u + 2 < 14 || nxt) ? 4 : 0) + ((u == 2 || (u == 12 && nxt)) ? 2 : 0);
        if constexpr (nr > 0) interleave<nr, 4>();
        SBAR();
      });
    });
  }
  float gs[2];
  {
    const unsigned hb = WS_H + (I * 4 + q) * 114688u + (2 * cw) * 1024u + late(lane) * 16u;
    const unsigned hp = hb + ((7 * j + 1) >> 1) * 8192u;
    const unsigned hs = hb + ((j & 1) ? ((7 * j) >> 1) * 8192u + 8u : (((7 * j) >> 1) + 3) * 8192u);
#pragma unroll
    for (int a = 0; a < 2; ++a) {
      const float L2E = 1.44269504088896341f;
      gs[a] = __builtin_amdgcn_rcpf(1.f + __builtin_amdgcn_exp2f(-L2E * lg[a]));
      const float inv = fminf(1.f + __builtin_amdgcn_exp2f(L2E * lg[a]), 1e30f);
      unsigned h2[7][2];
#pragma unroll
      for (int m = 0; m < 7; ++m) {
        float h[4];
#pragma unroll
        for (int r = 0; r < 4; ++r) {
          const float e = __builtin_amdgcn_exp2f(-L2E * hg[a][m][r]);
          h[r] = hu[a][m][r] * __builtin_amdgcn_rcpf(__builtin_fmaf(e, inv, inv));
        }
        h2[m][0] = pk2(h[0], h[1]);
        h2[m][1] = pk2(h[2], h[3]);
      }
#pragma unroll
      for (int v = 0; v < 3; ++v) {
        const u32x4 d = {h2[2 * v][0], h2[2 * v][1], h2[2 * v + 1][0], h2[2 * v + 1][1]};
        __builtin_amdgcn_raw_buffer_store_b128(d, rWs, hp + v * 8192 + a * 1024, 0, AUX_SC1);
      }
      const u32x2 d2 = {h2[6][0], h2[6][1]};
      __builtin_amdgcn_raw_buffer_store_b64(d2, rWs, hs + a * 1024, 0, AUX_SC1);
    }
  }

  f32x4 acc[2][7];
#pragma unroll
  for (int a = 0; a < 2; ++a)
#pragma unroll
    for (int m = 0; m < 7; ++m) acc[a][m] = f32x4{0, 0, 0, 0};
  {
    const int l3 = late(lane), g = l3 >> 4, c = l3 & 15;
    const int oT0 = (32 * cw + c) * 128 + ((g ^ (c >> 1)) << 4), oT1 = oT0 ^ 64;
    const int oE = 16384 + l3 * 16;
    const int oH = 2 * cw * 1024 + l3 * 16;
    const int oD = 16384 + (4 * g + (c >> 2)) * 224 + 8 * (c & 3);
    const unsigned myflag2 = WS_FLAG2 + ((I * 4 + q) * 4 + j) * 8u;
    bf16x8 ft[2][2], fe[2][7];
    auto rd = [&](auto N, auto H) {
      constexpr int n = N.value, h = H.value;
      LAS char* slot = lds + st_off<n>();
      if constexpr (n < N4) {
#pragma unroll
        for (int a = 0; a < 2; ++a) ft[h][a] = ldsr(slot + (h ? oT1 : oT0) + a * 2048);
#pragma unroll
        for (int m = 0; m < 7; ++m) fe[h][m] = ldsr(slot + oE + h * 7168 + m * 1024);
      } else {
#pragma unroll
        for (int a = 0; a < 2; ++a) ft[h][a] = ldsr(slot + oH + h * 8192 + a * 1024);
#pragma unroll
        for (int m = 0; m < 7; ++m) {
          LAS char* p = slot + oD + h * 7168 + m * 32;
          fe[h][m] = ldstr(p, p + 3584);
        }
      }
    };
    auto mm = [&](auto H) {
      constexpr int h = H.value;
#pragma unroll
      for (int m = 0; m < 7; ++m)
#pragma unroll
        for (int a = 0; a < 2; ++a) acc[a][m] = mfma(fe[h][m], ft[h][a], acc[a][m]);
    };
    rd(IC<N3>{}, IC<0>{});
    static_for<N3, NST>([&](auto N) {
      constexpr int n = N.value;
      rd(N, IC<1>{});
      mm(IC<0>{});
      interleave<(n < N4 ? 9 : 16), 14>();
      SBAR();
      if constexpr (n + 1 < NST) {
        WAITLGKM0();
        if constexpr (n + 1 == B_FLAG2) WAITVM(0);
        BAR();
        if constexpr (n + 1 == B_FLAG2) { if (cw == 0 && lane == 0) st_flag(ws, myflag2, tag); }
        rd(IC<n + 1>{}, IC<0>{});
      }
      mm(IC<1>{});
      if constexpr (n + 1 < NST) interleave<(n + 1 < N4 ? 9 : 16), 14>();
      SBAR();
      if constexpr (n + 1 == N4) {
#pragma unroll
        for (int a = 0; a < 2; ++a)
#pragma unroll
          for (int m = 0; m < 7; ++m) acc[a][m] *= gs[a];
        SBAR();
      }
    });
  }

  const int l4 = late(lane), g = l4 >> 4, c = l4 & 15;
  const unsigned inb = WS_INBOX + b * 32768u + cw * 2048u + l4 * 16u;
  f32x4 own[4];
#pragma unroll
  for (int k = 0; k < 4; ++k) own[k] = f32x4{0, 0, 0, 0};
  static_for<0, 4>([&](auto Q) {
    constexpr int qd = Q.value, a = qd & 1, mb = (qd >> 1) * 4;
    if (qd != q) {
      unsigned d[8];
#pragma unroll
      for (int k = 0; k < 4; ++k) {
        if (mb + k < 7) {
          const f32x4 x = acc[a][mb + k < 7 ? mb + k : 0];
          const unsigned u0 = pk2(x[0], x[1]), u1 = pk2(x[2], x[3]);
          d[2 * k] = u0 < SENT ? u0 : SENT - 1;
          d[2 * k + 1] = u1 < SENT ? u1 : SENT - 1;
        } else { d[2 * k] = 0; d[2 * k + 1] = 0; }
      }
      const unsigned o = WS_INBOX + ((b & ~3) | qd) * 32768u + q * 8192u + cw * 2048u + l4 * 16u;
      __builtin_amdgcn_raw_buffer_store_b128(u32x4{d[0], d[1], d[2], d[3]}, rWs, o, 0, AUX_SC1);
      __builtin_amdgcn_raw_buffer_store_b128(u32x4{d[4], d[5], d[6], d[7]}, rWs, o + 1024, 0, AUX_SC1);
    } else {
#pragma unroll
      for (int k = 0; k < 4; ++k) if (mb + k < 7) own[k] = acc[a][mb + k < 7 ? mb + k : 0];
    }
  });
  u32x4 in[6], sa[6], sb[6];
  const int mb = (q >> 1) * 4;
  unsigned ob = (128 * I + 32 * cw + 16 * (q & 1) + c) * 896u + (112 * j + 16 * mb + 4 * g) * 2u;
  {
    const unsigned o0 = inb + ((q + 1) & 3) * 8192u, o1 = inb + ((q + 2) & 3) * 8192u, o2 = inb + ((q + 3) & 3) * 8192u;
    unsigned left;
    asm volatile("" : "+v"(ob));
    asm volatile(
      "s_nop 4\n\t"
      "s_mov_b32 %[left], 0x100000\n\t"
      "buffer_load_dwordx4 v[128:131], %[o0], %[rs], 0 offen sc1\n\t"
      "buffer_load_dwordx4 v[132:135], %[o0], %[rs], 0 offen offset:1024 sc1\n\t"
      "buffer_load_dwordx4 v[136:139], %[o1], %[rs], 0 offen sc1\n\t"
      "buffer_load_dwordx4 v[140:143], %[o1], %[rs], 0 offen offset:1024 sc1\n\t"
      "buffer_load_dwordx4 v[144:147], %[o2], %[rs], 0 offen sc1\n\t"
      "buffer_load_dwordx4 v[148:151], %[o2], %[rs], 0 offen offset:1024 sc1\n\t"
      "s_sleep 22\n\t"
      ".Lfk_poll:\n\t"
      "buffer_load_dwordx4 v[152:155], %[o0], %[rs], 0 offen sc1\n\t"
      "buffer_load_dwordx4 v[156:159], %[o0], %[rs], 0 offen offset:1024 sc1\n\t"
      "buffer_load_dwordx4 v[160:163], %[o1], %[rs], 0 offen sc1\n\t"
      "buffer_load_dwordx4 v[164:167], %[o1], %[rs], 0 offen offset:1024 sc1\n\t"
      "buffer_load_dwordx4 v[168:171], %[o2], %[rs], 0 offen sc1\n\t"
      "buffer_load_dwordx4 v[172:175], %[o2], %[rs], 0 offen offset:1024 sc1\n\t"
      "s_waitcnt vmcnt(6)\n\t"
      "v_max3_u32 v200, v128, v129, v130\n\t"
      "v_max3_u32 v200, v200, v131, v132\n\t"
      "v_max3_u32 v200, v200, v133, v134\n\t"
      "v_max3_u32 v200, v200, v135, v136\n\t"
      "v_max3_u32 v200, v200, v137, v138\n\t"
      "v_max3_u32 v200, v200, v139, v140\n\t"
      "v_max3_u32 v200, v200, v141, v142\n\t"
      "v_max3_u32 v200, v200, v143, v144\n\t"
      "v_max3_u32 v200, v200, v145, v146\n\t"
      "v_max3_u32 v200, v200, v147, v148\n\t"
      "v_max3_u32 v200, v200, v149, v150\n\t"
      "v_max_u32 v200, v200, v151\n\t"
      "v_cmp_eq_u32 vcc, -1, v200\n\t"
      "s_cmp_eq_u64 vcc, 0\n\t"
      "s_cbranch_scc0 .Lfk_poll\n\t"
      "buffer_load_dwordx4 v[128:131], %[o0], %[rs], 0 offen sc1\n\t"
      "buffer_load_dwordx4 v[132:135], %[o0], %[rs], 0 offen offset:1024 sc1\n\t"
      "buffer_load_dwordx4 v[136:139], %[o1], %[rs], 0 offen sc1\n\t"
      "buffer_load_dwordx4 v[140:143], %[o1], %[rs], 0 offen offset:1024 sc1\n\t"
      "buffer_load_dwordx4 v[144:147], %[o2], %[rs], 0 offen sc1\n\t"
      "buffer_load_dwordx4 v[148:151], %[o2], %[rs], 0 offen offset:1024 sc1\n\t"
      "s_waitcnt vmcnt(6)\n\t"
      "v_max3_u32 v200, v152, v153, v154\n\t"
      "v_max3_u32 v200, v200, v155, v156\n\t"
      "v_max3_u32 v200, v200, v157, v158\n\t"
      "v_max3_u32 v200, v200, v159, v160\n\t"
      "v_max3_u32 v200, v200, v161, v162\n\t"
      "v_max3_u32 v200, v200, v163, v164\n\t"
      "v_max3_u32 v200, v200, v165, v166\n\t"
      "v_max3_u32 v200, v200, v167, v168\n\t"
      "v_max3_u32 v200, v200, v169, v170\n\t"
      "v_max3_u32 v200, v200, v171, v172\n\t"
      "v_max3_u32 v200, v200, v173, v174\n\t"
      "v_max_u32 v200, v200, v175\n\t"
      "v_cmp_eq_u32 vcc, -1, v200\n\t"
      "s_cmp_eq_u64 vcc, 0\n\t"
      "s_cbranch_scc0 .Lfk_poll\n\t"
      "v_mov_b64 v[176:177], v[152:153]\n\t"
      "v_mov_b64 v[178:179], v[154:155]\n\t"
      "v_mov_b64 v[180:181], v[156:157]\n\t"
      "v_mov_b64 v[182:183], v[158:159]\n\t"
      "v_mov_b64 v[184:185], v[160:161]\n\t"
      "v_mov_b64 v[186:187], v[162:163]\n\t"
      "v_mov_b64 v[188:189], v[164:165]\n\t"
      "v_mov_b64 v[190:191], v[166:167]\n\t"
      "v_mov_b64 v[192:193], v[168:169]\n\t"
      "v_mov_b64 v[194:195], v[170:171]\n\t"
      "v_mov_b64 v[196:197], v[172:173]\n\t"
      "v_mov_b64 v[198:199], v[174:175]\n\t"
      "s_branch .Lfk_poll_end\n"
      ".Lfk_poll_0:\n\t"
      "v_mov_b64 v[176:177], v[128:129]\n\t"
      "v_mov_b64 v[178:179], v[130:131]\n\t"
      "v_mov_b64 v[180:181], v[132:133]\n\t"
      "v_mov_b64 v[182:183], v[134:135]\n\t"
      "v_mov_b64 v[184:185], v[136:137]\n\t"
      "v_mov_b64 v[186:187], v[138:139]\n\t"
      "v_mov_b64 v[188:189], v[140:141]\n\t"
      "v_mov_b64 v[190:191], v[142:143]\n\t"
      "v_mov_b64 v[192:193], v[144:145]\n\t"
      "v_mov_b64 v[194:195], v[146:147]\n\t"
      "v_mov_b64 v[196:197], v[148:149]\n\t"
      "v_mov_b64 v[198:199], v[150:151]\n\t"
      "s_branch .Lfk_poll_end\n"
      ".Lfk_poll_end:"
      : "=&{v[176:179]}"(in[0]), "=&{v[180:183]}"(in[1]), "=&{v[184:187]}"(in[2]), "=&{v[188:191]}"(in[3]), "=&{v[192:195]}"(in[4]), "=&{v[196:199]}"(in[5]),
        "=&{v[128:131]}"(sa[0]), "=&{v[132:135]}"(sa[1]), "=&{v[136:139]}"(sa[2]), "=&{v[140:143]}"(sa[3]), "=&{v[144:147]}"(sa[4]), "=&{v[148:151]}"(sa[5]),
        "=&{v[152:155]}"(sb[0]), "=&{v[156:159]}"(sb[1]), "=&{v[160:163]}"(sb[2]), "=&{v[164:167]}"(sb[3]), "=&{v[168:171]}"(sb[4]), "=&{v[172:175]}"(sb[5]),
        [left] "=&s"(left)
      : [o0] "v"(o0), [o1] "v"(o1), [o2] "v"(o2), [rs] "s"(rWs)
      : "v200", "vcc", "scc", "memory");
  }
  {
    const rsrc_t rO = mkrs(out, 2048u * 896u);
#pragma unroll
    for (int k = 0; k < 4; ++k) {
      f32x4 s = own[k];
#pragma unroll
      for (int d = 0; d < 3; ++d) {
        const unsigned u0 = in[2 * d + (k >> 1)][2 * (k & 1)], u1 = in[2 * d + (k >> 1)][2 * (k & 1) + 1];
        s += f32x4{bflo(u0), bfhi(u0), bflo(u1), bfhi(u1)};
      }
      if (k < 3 || mb == 0) {
        const u32x2 o2 = {pk2(s[0], s[1]), pk2(s[2], s[3])};
        __builtin_amdgcn_raw_buffer_store_b64(o2, rO, ob + 32 * k, 0, 0);
      }
    }
  }
  asm volatile("s_waitcnt vmcnt(3)"
      :: "{v[128:131]}"(sa[0]), "{v[132:135]}"(sa[1]), "{v[136:139]}"(sa[2]), "{v[140:143]}"(sa[3]), "{v[144:147]}"(sa[4]), "{v[148:151]}"(sa[5]),
         "{v[152:155]}"(sb[0]), "{v[156:159]}"(sb[1]), "{v[160:163]}"(sb[2]), "{v[164:167]}"(sb[3]), "{v[168:171]}"(sb[4]), "{v[172:175]}"(sb[5])
      : "memory");
}

__global__ __launch_bounds__(512, 2) void fused_kernel(const void* Ax, const void* Cre, const void* Cim, const void* Wdre, const void* Wdim,
                                                       const void* Wg, const void* Wu, const void* Wdn, const void* gw, void* out, char* ws) {
  __shared__ __attribute__((aligned(1024))) char shm[LDS_BYTES];
  LAS char* lds = (LAS char*)shm;
  {
    SBAR();
    u64 pc;
    unsigned len, o0, o1, oc;
    asm volatile("s_getpc_b64 %0\n.Lfk_entry:\n\ts_mov_b32 %1, .Lfk_code_end-.Lfk_entry\n\ts_mov_b32 %2, .Lfk_loader0-.Lfk_entry-256\n\t"
                 "s_mov_b32 %3, .Lfk_loader1-.Lfk_entry-256\n\ts_mov_b32 %4, .Lfk_loaders-.Lfk_entry"
                 : "=s"(pc), "=s"(len), "=s"(o0), "=s"(o1), "=s"(oc));
    const unsigned w = threadIdx.x >> 6, l = threadIdx.x & 63;
    const unsigned o = w < 2 && l < 36 ? (l < 4 ? oc : w ? o1 : o0) + l * 64u : 0x80000000u;
    __builtin_amdgcn_raw_ptr_buffer_load_lds(mkrs((const void*)pc, len), (LAS void*)(lds + PF_OFF), 4, o, 0, 0, 0);
    SBAR();
  }
  asm volatile("" : "+s"(Ax), "+s"(Cre), "+s"(Cim), "+s"(Wdre), "+s"(Wdim), "+s"(Wg), "+s"(Wu), "+s"(Wdn), "+s"(gw), "+s"(out), "+s"(ws));
  Geo G;
  G.lane = threadIdx.x & 63;
  G.wid = __builtin_amdgcn_readfirstlane(threadIdx.x >> 6);
  G.b = blockIdx.x;
  G.q = G.b & 3;
  G.r = (G.b >> 2) & 1;
  G.j = (G.b >> 3) & 3;
  G.i = G.b >> 5;
  G.I = 8 * G.r + G.i;
  G.k7 = (G.i + 2 * G.j) % 7;
  G.k16 = 2 * G.i;
  if (G.wid >= 4) {
    asm volatile(".Lfk_loaders:");
    SBAR();
    const bool im = G.q >= 2;
    const void* Cp = im ? Cim : Cre;
    const void* Wdp = im ? Wdim : Wdre;
    if (G.wid < 6) loader<0>(G, lds, ws, Ax, Cp, Wdp, Wg, Wu, Wdn);
    else loader<1>(G, lds, ws, Ax, Cp, Wdp, Wg, Wu, Wdn);
  } else {
    compute(G, lds, ws, gw, out, Wg, Wu, Wdn, G.q >= 2 ? Cim : Cre);
  }
  asm volatile(".Lfk_code_end:");
}

}

extern "C" void kernel_launch(void* const* d_in, const int* in_sizes, int n_in, void* d_out, int out_size, void* d_ws,
                              size_t ws_size, hipStream_t stream) {
  (void)in_sizes; (void)n_in; (void)out_size; (void)ws_size;
  fk::fused_kernel<<<256, 512, 0, stream>>>(d_in[0], d_in[1], d_in[2], d_in[3], d_in[4], d_in[6], d_in[7], d_in[8], d_in[9],
                                            d_out, (char*)d_ws);
}
```

```cpp
#include <hip/hip_runtime.h>
#include <hip/hip_bf16.h>
#include <stdint.h>

#define LAS __attribute__((address_space(3)))
#define GAS __attribute__((address_space(1)))

namespace fk {

typedef __bf16 bf16x2 __attribute__((ext_vector_type(2)));
typedef __bf16 bf16x4 __attribute__((ext_vector_type(4)));
typedef __bf16 bf16x8 __attribute__((ext_vector_type(8)));
typedef float f32x2 __attribute__((ext_vector_type(2)));
typedef float f32x4 __attribute__((ext_vector_type(4)));
typedef unsigned u32x2 __attribute__((ext_vector_type(2)));
typedef unsigned u32x4 __attribute__((ext_vector_type(4)));
typedef unsigned long long u64;
typedef __amdgpu_buffer_rsrc_t rsrc_t;

template <int V> struct IC { static constexpr int value = V; };
template <int B, int E, class Fn> __device__ __forceinline__ void static_for(Fn&& f) {
  if constexpr (B < E) { f(IC<B>{}); static_for<B + 1, E>(f); }
}

constexpr int NST = 37, N2 = 7, N3 = 14, N4 = 30;
constexpr int SZ1 = 22528, SZ2 = 45056, SZ3 = 30720;
constexpr int RING = 161792, GW_OFF = RING, PF_OFF = RING + 1024, LDS_BYTES = RING + 2048;
constexpr int st_size(int n) { return n < N2 ? SZ1 : (n < N3 ? SZ2 : SZ3); }
constexpr int st_szA(int n) { return n < N2 ? SZ1 : 16384; }
constexpr int cntA(int cls, int n) { return n < N2 ? (cls ? 5 : 6) : 4; }
constexpr int cntB(int cls, int n) { return n < N2 ? 0 : (n < N3 ? 7 : (cls ? 3 : 4)); }
constexpr int N_GAP = 19, T_GAP = 17;
struct Plan { int off[NST]; int atA[NST]; int atB[NST]; };
constexpr Plan make_plan() {
  Plan p{};
  constexpr int ova[10] = {-1, -1, 0, 0, 1, 2, 3, 4, 5, 7};
  constexpr int ovb[3] = {5, 6, 8};
  int cur = 0;
  for (int n = 0; n < NST; ++n) {
    if (cur + st_size(n) > RING) cur = 0;
    p.off[n] = cur;
    cur += st_size(n);
    int a = -1, b = -1;
    const int lo = p.off[n], mid = lo + st_szA(n), hi = lo + st_size(n);
    for (int m = 0; m < n; ++m) {
      const int ml = p.off[m], mh = ml + st_size(m);
      if (ml < mid && lo < mh) a = m + 1;
      if (mid < hi && ml < hi && mid < mh) b = m + 1;
    }
    if (n < 10 && a < ova[n]) a = ova[n];
    if (n >= N2 && n < 10 && b < ovb[n - N2]) b = ovb[n - N2];
    if (n >= 10) { if (a < b) a = b; b = a; }
    if (n > 0 && a < p.atA[n - 1]) a = p.atA[n - 1];
    if (n > N2 && b < p.atB[n - 1]) b = p.atB[n - 1];
    if (n == N_GAP && a < T_GAP) a = T_GAP;
    if ((n > N_GAP || (n > N3 && n < N3 + 3)) && a <= p.atA[n - 1]) a = p.atA[n - 1] + 1;
    if (n >= 10 && b < a) b = a;
    if (n >= 10 && a < b) a = b;
    p.atA[n] = a;
    p.atB[n] = n < N2 ? a : b;
  }
  return p;
}
constexpr Plan PLAN = make_plan();
template <int N> constexpr int st_off() { return PLAN.off[N]; }
template <int N> constexpr int st_atA() { return PLAN.atA[N]; }
template <int N> constexpr int st_atB() { return PLAN.atB[N]; }
constexpr int T_EPOCH = -1;
constexpr int T_PF1 = PLAN.atA[N3] - 2, T_PF2 = PLAN.atA[N4] - 2;
constexpr int T_C0 = 7, T_C1 = 9, T_C2 = 11, T_C3 = 13;
constexpr int T_D = 20;
constexpr int pf_cnt(int a) {
  return (a == T_C0) + (a == T_C1) + (a == T_C2) + (a == T_C3) + (a == T_C0 + 1) + (a == T_C1 + 1) + (a == T_C2 + 1) + (a == T_C3 + 1) +
         (a == T_D) + (a == T_EPOCH) + (a == T_PF1) + (a == T_PF2);
}
constexpr int st_wait_c(int cls, int n) {
  int c = 0;
  bool seenA = false, seenB = false;
  for (int t = -1; t <= n - 1; ++t) {
    for (int s = 0; s < NST; ++s) {
      if (PLAN.atA[s] == t) { if (s == n) { seenA = true; c = 0; } else c += cntA(cls, s); }
      if (PLAN.atB[s] == t && cntB(cls, s) > 0) { if (s == n) { seenB = true; c = 0; } else c += cntB(cls, s); }
    }
    c += pf_cnt(t);
  }
  return seenA && (seenB || cntB(cls, n) == 0) ? c : -1;
}
template <int CLS, int N> constexpr int st_wait() { return st_wait_c(CLS, N); }
constexpr bool plan_ok() {
  for (int n = 0; n < NST; ++n)
    for (int cls = 0; cls < 2; ++cls)
      if (st_wait_c(cls, n) < 0 || st_wait_c(cls, n) > 62) return false;
  return true;
}
static_assert(plan_ok(), "stage plan");

constexpr int B_TAG = 4;
constexpr int B_FLAG1 = 9;
constexpr int N_POLL1 = 11;
constexpr int B_FLAG2 = 21;

constexpr unsigned WS_EPOCH = 0;
constexpr unsigned WS_FLAG1 = 4096;
constexpr unsigned WS_FLAG2 = 8192;
constexpr unsigned WS_LOGIT = 16384;
constexpr unsigned WS_ABT = 131072;
constexpr unsigned WS_H = 4u << 20;
constexpr unsigned WS_INBOX = 12u << 20;
constexpr unsigned WS_END = 20u << 20;
constexpr unsigned SENT = 0xFFFFFFFFu;
constexpr int SPIN_MAX = 1 << 20;
constexpr int AUX_SC1 = 16;

#define BAR() do { asm volatile("" ::: "memory"); __builtin_amdgcn_s_barrier(); asm volatile("" ::: "memory"); } while (0)
#define WAITVM(n) asm volatile("s_waitcnt vmcnt(%0)" ::"n"(n) : "memory")
#define WAITLGKM0() do { __builtin_amdgcn_s_waitcnt(0xC07F); asm volatile("" ::: "memory"); } while (0)
#define SBAR() __builtin_amdgcn_sched_barrier(0)

__device__ __forceinline__ rsrc_t mkrs(const void* p, unsigned bytes) {
  return __builtin_amdgcn_make_buffer_rsrc((void*)p, 0, (int)bytes, 0x00020000);
}
template <int AUX> __device__ __forceinline__ void dma(rsrc_t rs, unsigned voff, unsigned soff, LAS char* l) {
  __builtin_amdgcn_raw_ptr_buffer_load_lds(rs, (LAS void*)l, 16, voff, soff, 0, AUX);
}
__device__ __forceinline__ bf16x8 ldsr(LAS char* p) { return *(LAS bf16x8*)p; }
__device__ __forceinline__ bf16x8 ldstr(LAS char* p0, LAS char* p1) {
  bf16x4 a = __builtin_amdgcn_ds_read_tr16_b64_v4bf16((LAS bf16x4*)p0);
  bf16x4 b = __builtin_amdgcn_ds_read_tr16_b64_v4bf16((LAS bf16x4*)p1);
  return __builtin_shufflevector(a, b, 0, 1, 2, 3, 4, 5, 6, 7);
}
__device__ __forceinline__ f32x4 mfma(bf16x8 a, bf16x8 b, f32x4 c) { return __builtin_amdgcn_mfma_f32_16x16x32_bf16(a, b, c, 0, 0, 0); }
__device__ __forceinline__ unsigned pk2(float x, float y) {
  f32x2 v = {x, y};
  return __builtin_bit_cast(unsigned, __builtin_convertvector(v, bf16x2));
}
__device__ __forceinline__ float bflo(unsigned d) { return __builtin_bit_cast(float, d << 16); }
__device__ __forceinline__ float bfhi(unsigned d) { return __builtin_bit_cast(float, d & 0xFFFF0000u); }
__device__ __forceinline__ u64 ld_flag(const char* ws, unsigned off) {
  return __hip_atomic_load((const GAS u64*)(ws + off), __ATOMIC_RELAXED, __HIP_MEMORY_SCOPE_AGENT);
}
__device__ __forceinline__ void st_flag(char* ws, unsigned off, u64 v) {
  __hip_atomic_store((GAS u64*)(ws + off), v, __ATOMIC_RELAXED, __HIP_MEMORY_SCOPE_AGENT);
}
__device__ __forceinline__ u64 make_tag(u64 epoch) {
  const unsigned lo = __builtin_amdgcn_readfirstlane((unsigned)epoch), hi = __builtin_amdgcn_readfirstlane((unsigned)(epoch >> 32));
  return (((u64)hi << 32) | lo) * 6364136223846793005ull + 1442695040888963407ull;
}
__device__ __forceinline__ void wait_flag(const char* ws, unsigned off, u64 v, u64 tag) {
  asm volatile("" : "+v"(v));
  if (__builtin_amdgcn_ballot_w64(v != tag) != 0) {
    for (int it = 0; it < SPIN_MAX; ++it) {
      __builtin_amdgcn_s_sleep(2);
      if (__builtin_amdgcn_ballot_w64(ld_flag(ws, off) != tag) == 0) break;
    }
  }
  asm volatile("" ::: "memory");
}
template <int NR, int NM> __device__ __forceinline__ void interleave() {
  static_for<0, NM>([&](auto K) {
    constexpr int k = K.value, cnt = NR / NM + (k < NR % NM ? 1 : 0);
    __builtin_amdgcn_sched_group_barrier(0x008, 1, 0);
    if constexpr (cnt > 0) __builtin_amdgcn_sched_group_barrier(0x100, cnt, 0);
  });
}

__device__ __forceinline__ int late(int x) { asm volatile("" : "+v"(x)); return x; }

struct Geo { int b, r, q, i, j, I, lane, wid, k7, k16; };
template <int NK> __device__ __forceinline__ unsigned kstep(const Geo& G, int k) {
  const unsigned x = (NK == 7 ? G.k7 : G.k16) + k;
  return x >= NK ? x - NK : x;
}

template <int CLS>
__device__ __forceinline__ void loader(const Geo& G, LAS char* lds, char* ws, const void* Ax, const void* Cp, const void* Wdp,
                                       const void* Wg, const void* Wu, const void* Wdn) {
  const int lane = G.lane, lw = G.wid - 4, q = G.q, j = G.j, I = G.I;
  asm volatile(".Lfk_loader%0:" ::"n"(CLS));
  SBAR();
  const rsrc_t rA = mkrs(Ax, 2048u * 896u), rWd = mkrs(Wdp, 448u * 896u);
  constexpr int NB = CLS ? 3 : 4;
  const int blk0 = CLS ? 8 + 3 * (lw - 2) : 4 * lw;
  unsigned vA[2], vWd[NB], vX[4], vW[7], vC[4], vWdn[NB];
  {
    const int r8 = lane >> 3, ch = lane & 7;
    const int sig = 2 * I + (q & 1);
#pragma unroll
    for (int n = 0; n < 2; ++n) {
      const int row = 16 * lw + 8 * n + r8;
      const int f = ((row >> 1) & 1) | (((row >> 3) & 3) << 1);
      vA[n] = (64 * sig + row) * 896u + ((ch ^ f) << 4);
    }
#pragma unroll
    for (int n = 0; n < NB; ++n) {
      const int row = 8 * (blk0 + n) + r8;
      vWd[n] = (112 * j + row) * 896u + ((ch ^ ((row >> 1) & 7)) << 4);
    }
  }
  const rsrc_t rC = mkrs(Cp, 2048u * 4096u), rWs = mkrs(ws, WS_END), rWdn = mkrs(Wdn, 1792u * 896u);
  const rsrc_t rW = mkrs(lw < 2 ? Wg : Wu, 448u * 3584u);
  unsigned vAB, vH, fo1, fo2, tD, tC, tK;
  u64 tag = 0, f1 = 0, f2 = 0;

  auto issueA = [&](auto S) {
    constexpr int s = S.value;
    LAS char* slot = lds + st_off<s>();
    if constexpr (s < N2) {
#pragma unroll
      for (int n = 0; n < 2; ++n) dma<0>(rA, vA[n], 128 * kstep<7>(G, s), slot + (2 * lw + n) * 1024);
#pragma unroll
      for (int n = 0; n < NB; ++n) dma<0>(rWd, vWd[n], 128 * kstep<7>(G, s), slot + 8192 + (blk0 + n) * 1024);
    } else if constexpr (s < N3) {
      const unsigned k = kstep<7>(G, s - N2);
#pragma unroll
      for (int n = 0; n < 4; ++n) dma<0>(rA, vX[n], 128 * k, slot + (4 * lw + n) * 1024);
    } else if constexpr (s < N4) {
      const unsigned k = kstep<16>(G, s - N3);
      if constexpr (s == N3) wait_flag(ws, fo1, f1, tag);
#pragma unroll
      for (int n = 0; n < 4; ++n) dma<0>(rC, vC[n], 128 * k, slot + (4 * lw + n) * 1024);
    } else {
      const unsigned k = kstep<7>(G, s - N4);
      if constexpr (s == N4) wait_flag(ws, fo2, f2, tag);
#pragma unroll
      for (int n = 0; n < 4; ++n) dma<AUX_SC1>(rWs, vH, 16384 * k + 1024 * n, slot + (4 * lw + n) * 1024);
    }
  };
  auto issueB = [&](auto S) {
    constexpr int s = S.value;
    LAS char* slot = lds + st_off<s>();
    if constexpr (s >= N2 && s < N3) {
      const unsigned k = kstep<7>(G, s - N2);
#pragma unroll
      for (int n = 0; n < 7; ++n) dma<0>(rW, vW[n], 229376 * k, slot + 16384 + (7 * lw + n) * 1024);
    } else if constexpr (s >= N3 && s < N4) {
      const unsigned k = kstep<16>(G, s - N3);
#pragma unroll
      for (int n = 0; n < NB; ++n) dma<AUX_SC1>(rWs, vAB, 14336 * k + 1024 * n, slot + 16384 + (blk0 + n) * 1024);
    } else if constexpr (s >= N4) {
      const unsigned k = kstep<7>(G, s - N4);
#pragma unroll
      for (int n = 0; n < NB; ++n) dma<0>(rWdn, vWdn[n], 57344 * k, slot + 16384 + (blk0 + n) * 1024);
    }
  };
  auto issue_at = [&](auto T) {
    static_for<0, NST>([&](auto S) {
      if constexpr (st_atA<S.value>() == T.value) issueA(S);
      if constexpr (st_atB<S.value>() == T.value) issueB(S);
    });
  };
  auto touch = [&](rsrc_t rs, unsigned voff, unsigned soff) {
    __builtin_amdgcn_raw_ptr_buffer_load_lds(rs, (LAS void*)(lds + PF_OFF), 4, voff, soff, 0, 0);
  };
  auto vpf = [&](auto A) {
    constexpr int a = A.value;
    auto touchC = [&](int e, int par) {
      const unsigned o = tC + (((tK + 4 * e) & 15) << 7);
      touch(rC, (G.i & 1) == par ? o : 0x80000000u, 0);
    };
    if constexpr (a == T_C0) touchC(0, 0);
    if constexpr (a == T_C1) touchC(1, 0);
    if constexpr (a == T_C2) touchC(2, 0);
    if constexpr (a == T_C3) touchC(3, 0);
    if constexpr (a == T_C0 + 1) touchC(0, 1);
    if constexpr (a == T_C1 + 1) touchC(1, 1);
    if constexpr (a == T_C2 + 1) touchC(2, 1);
    if constexpr (a == T_C3 + 1) touchC(3, 1);
    if constexpr (a == T_D) touch(rWdn, tD, 0);
    if constexpr (a == T_PF1) f1 = ld_flag(ws, fo1);
    if constexpr (a == T_PF2) f2 = ld_flag(ws, fo2);
  };
  issue_at(IC<-1>{});
  const int ln = late(lane);
  u64 epoch = ld_flag(ws, WS_EPOCH);
  {
    const int r8 = ln >> 3, ch = ln & 7;
#pragma unroll
    for (int n = 0; n < 4; ++n) {
      const int row = 32 * lw + 8 * n + r8;
      const unsigned sw = (ch ^ ((row >> 1) & 7)) << 4;
      vX[n] = (128 * I + row) * 896u + sw;
      vC[n] = (128 * I + row) * 4096u + sw + 2048u * (q & 1);
    }
#pragma unroll
    for (int n = 0; n < 7; ++n) {
      const int idx = 7 * lw + n, ii = idx >= 14 ? idx - 14 : idx;
      const unsigned c16 = ii * 64 + ln, pr = (c16 * 4682u) >> 16, cc = c16 - 14u * pr;
      const unsigned rho = (pr & ~12u) | ((pr & 8) >> 1) | ((pr & 4) << 1);
      vW[n] = rho * 3584u + cc * 16u + (448u * q + 112u * j) * 2u;
    }
#pragma unroll
    for (int n = 0; n < NB; ++n) {
      const unsigned c16 = (blk0 + n) * 64 + ln, pr = (c16 * 4682u) >> 16, cc = c16 - 14u * pr;
      vWdn[n] = (448 * q + pr) * 896u + 224u * j + cc * 16u;
    }
    vAB = WS_ABT + (q * 4 + j) * 16u * 14336u + blk0 * 1024u + ln * 16u;
    vH = WS_H + (I * 4 + q) * 114688u + lw * 4096u + ln * 16u;
    fo1 = WS_FLAG1 + ((q * 4 + j) * 16 + (ln & 15)) * 8u;
    fo2 = WS_FLAG2 + ((I * 4 + q) * 4 + (ln & 3)) * 8u;
    const unsigned pid = ((G.i * 4 + j) * 4 + lw) * 49 + ln;
    tD = ln < 49 ? 448u * 896u * q + pid * 64u : 0x80000000u;
    tC = (128 * I + 32 * j + 8 * lw + ((ln >> 1) & 7)) * 4096u + 2048u * (q & 1) + (ln & 1) * 64u;
    tK = kstep<16>(G, 0) + (ln >> 4);
  }
  vpf(IC<-1>{});
  static_for<0, NST>([&](auto N) {
    constexpr int n = N.value;
    WAITVM((st_wait<CLS, n>()));
    BAR();
    if constexpr (n == B_TAG) {
      asm volatile("" : "+v"(epoch));
      tag = make_tag(epoch);
    }
    issue_at(N);
    vpf(N);
  });
  if (G.b == 0 && lw == 3) {
#pragma unroll
    for (int k = 0; k < 4; ++k) {
      const unsigned fo = WS_FLAG1 + (k * 64 + lane) * 8u;
      wait_flag(ws, fo, ld_flag(ws, fo), tag);
    }
    if (lane == 0) st_flag(ws, WS_EPOCH, tag);
  }
}

__device__ __forceinline__ void clear_inbox(const Geo& G, rsrc_t rWs) {
#pragma unroll
  for (int d = 1; d < 4; ++d) {
    const u32x4 s4 = {SENT, SENT, SENT, SENT};
    const unsigned o = WS_INBOX + G.b * 32768u + G.wid * 2048u + late(G.lane) * 16u + ((G.q + d) & 3) * 8192u;
    __builtin_amdgcn_raw_buffer_store_b128(s4, rWs, o, 0, AUX_SC1);
    __builtin_amdgcn_raw_buffer_store_b128(s4, rWs, o + 1024, 0, AUX_SC1);
  }
}
template <int EH>
__device__ __forceinline__ void g1_phase(const Geo& G, LAS char* lds, char* ws, rsrc_t rWs) {
  const int lane = G.lane, cw = G.wid, g = lane >> 4, c = lane & 15, sh = cw >> 1;
  f32x4 acc[2][4];
#pragma unroll
  for (int a = 0; a < 2; ++a)
#pragma unroll
    for (int k = 0; k < 4; ++k) acc[a][k] = f32x4{0, 0, 0, 0};
  const int fA = ((c >> 1) & 1) | ((c >> 2) << 1);
  const int oA0 = (32 * sh + 8 * (c >> 2) + (c & 3)) * 128 + ((g ^ fA) << 4), oA1 = oA0 ^ 64;
  const int oB0 = 8192 + (64 * EH + c) * 128 + ((g ^ (c >> 1)) << 4), oB1 = oB0 ^ 64;
  const int oG = GW_OFF + 16 * g;
  bf16x8 fa[2][2], fb[2][4];
  auto rd = [&](auto N, auto H) {
    constexpr int n = N.value, h = H.value;
    LAS char* slot = lds + st_off<n>();
#pragma unroll
    for (int a = 0; a < 2; ++a) fa[h][a] = ldsr(slot + (h ? oA1 : oA0) + a * 512);
#pragma unroll
    for (int k = 0; k < 4; ++k) {
      if (EH && k == 3) fb[h][k] = ldsr(lds + (oG + 128 * kstep<7>(G, n)) + 64 * h);
      else fb[h][k] = ldsr(slot + (h ? oB1 : oB0) + k * 2048);
    }
  };
  auto mm = [&](auto H) {
    constexpr int h = H.value;
#pragma unroll
    for (int k = 0; k < 4; ++k)
#pragma unroll
      for (int a = 0; a < 2; ++a) acc[a][k] = mfma(fa[h][a], fb[h][k], acc[a][k]);
  };
  BAR();
  rd(IC<0>{}, IC<0>{});
  static_for<0, N2>([&](auto N) {
    constexpr int n = N.value;
    rd(N, IC<1>{});
    mm(IC<0>{});
    interleave<6, 8>();
    SBAR();
    WAITLGKM0();
    BAR();
    if constexpr (n == 0) clear_inbox(G, rWs);
    if constexpr (n + 1 < N2) rd(IC<n + 1>{}, IC<0>{});
    mm(IC<1>{});
    if constexpr (n + 1 < N2) interleave<6, 8>();
    SBAR();
  });
  const int p = G.q >> 1;
  const unsigned base = WS_ABT + (((2 * p + G.r) * 4 + G.j) * 16 + 2 * G.i + (G.q & 1)) * 14336u + sh * 7168u + EH * 4096u + lane * 16u;
  const float sg = p ? -1.f : 1.f;
#pragma unroll
  for (int k = 0; k < (EH ? 3 : 4); ++k) {
    const f32x4 x = acc[0][k] * sg, y = acc[1][k] * sg;
    const u32x4 v = {pk2(x[0], x[1]), pk2(x[2], x[3]), pk2(y[0], y[1]), pk2(y[2], y[3])};
    __builtin_amdgcn_raw_buffer_store_b128(v, rWs, base + k * 1024, 0, AUX_SC1);
  }
  if (EH && c == 0) {
    const unsigned lo = WS_LOGIT + G.b * 256u + (32 * sh + 8 * g) * 4u;
    __builtin_amdgcn_raw_buffer_store_b128(__builtin_bit_cast(u32x4, acc[0][3]), rWs, lo, 0, AUX_SC1);
    __builtin_amdgcn_raw_buffer_store_b128(__builtin_bit_cast(u32x4, acc[1][3]), rWs, lo + 16, 0, AUX_SC1);
  }
}

__device__ __forceinline__ void compute(const Geo& G, LAS char* lds, char* ws, const void* gw, void* out, const void* Wg,
                                        const void* Wu, const void* Wdn, const void* Cp) {
  const int lane = G.lane, cw = G.wid, q = G.q, j = G.j, I = G.I, b = G.b;
  const u64 epoch = ld_flag(ws, WS_EPOCH);
  const rsrc_t rWs = mkrs(ws, WS_END);
  {
    u64 pc;
    unsigned len;
    asm volatile("s_getpc_b64 %0\n.Lfk_code_begin:\n\ts_mov_b32 %1, .Lfk_code_end-.Lfk_code_begin" : "=s"(pc), "=s"(len));
    const unsigned o = lane < 7 ? (((b >> 3) * 4 + cw) * 7 + lane) * 64u : 0x80000000u;
    __builtin_amdgcn_raw_ptr_buffer_load_lds(mkrs((const void*)pc, len), (LAS void*)(lds + PF_OFF), 4, o, 0, 0, 0);
  }
  if (cw == 2) {
    const unsigned last = lane & 1;
    const char* pa = (const char*)Wg + last * (448u * 3584u - 4);
    if ((lane >> 1) == 1) pa = (const char*)Wu + last * (448u * 3584u - 4);
    if ((lane >> 1) == 2) pa = (const char*)Wdn + 401408u * q + last * 401404u;
    if ((lane >> 1) == 3) pa = (const char*)Cp + 524288u * I + last * 524284u;
    if (lane < 8) __builtin_amdgcn_global_load_lds((const GAS void*)pa, (LAS void*)(lds + PF_OFF + 512), 4, 0, 0);
  }
  if (cw == 1) {
    const rsrc_t rG = mkrs(gw, 896u);
    const u32x4 v = __builtin_amdgcn_raw_buffer_load_b128(rG, lane * 16u, 0, 0);
    *(LAS u32x4*)(lds + GW_OFF + lane * 16) = v;
    WAITLGKM0();
  }
  __builtin_amdgcn_s_waitcnt(0x0F70);
  const u64 tag = make_tag(epoch);
  if (cw & 1) g1_phase<1>(G, lds, ws, rWs);
  else g1_phase<0>(G, lds, ws, rWs);

  const unsigned myflag1 = WS_FLAG1 + (((2 * (q >> 1) + G.r) * 4 + j) * 16 + 2 * G.i + (q & 1)) * 8u;
  float lg[2];
  f32x4 hg[2][7], hu[2][7];
#pragma unroll
  for (int a = 0; a < 2; ++a)
#pragma unroll
    for (int m = 0; m < 7; ++m) { hg[a][m] = f32x4{0, 0, 0, 0}; hu[a][m] = f32x4{0, 0, 0, 0}; }
  {
    const int l2 = late(lane), g = l2 >> 4, c = l2 & 15;
    const int oT0 = (32 * cw + c) * 128 + ((g ^ (c >> 1)) << 4), oT1 = oT0 ^ 64;
    const int oWb = 16384 + (16 * (g >> 1) + 4 * (g & 1) + (c >> 2)) * 224 + 8 * (c & 3);
    const int oWP = oWb + 32 * (j & 1), oWS = oWb + ((j & 1) ? 0 : 192);
    bf16x8 t[2][2], wg[3], wu[3];
    u64 fv = 0;
    auto rdW = [&](auto N, auto U) {
      constexpr int n = N.value, u = U.value, kap = u / 7, m = u % 7, bi = (14 * (n - N2) + u) % 3;
      LAS char* p = lds + st_off<n>() + (m < 6 ? oWP + 32 * m : oWS) + kap * 7168;
      wg[bi] = ldstr(p, p + 1792);
      wu[bi] = ldstr(p + 14336, p + 14336 + 1792);
    };
    auto rdT = [&](auto N, auto H) {
      constexpr int n = N.value, h = H.value;
#pragma unroll
      for (int a = 0; a < 2; ++a) t[h][a] = ldsr(lds + st_off<n>() + (h ? oT1 : oT0) + a * 2048);
    };
    rdT(IC<N2>{}, IC<0>{});
    rdW(IC<N2>{}, IC<0>{});
    rdW(IC<N2>{}, IC<1>{});
    SBAR();
    static_for<N2, N3>([&](auto N) {
      constexpr int n = N.value;
      if constexpr (n == N_POLL1 || n == N_POLL1 + 1) {
        const int lp = late(lane), qq = lp & 3;
        const unsigned fo4 = WS_FLAG1 + (((2 * (qq >> 1) + G.r) * 4 + j) * 16 + 2 * G.i + (qq & 1)) * 8u;
        if constexpr (n == N_POLL1) fv = ld_flag(ws, fo4);
        else wait_flag(ws, fo4, fv, tag);
      }
      if constexpr (n == N_POLL1 + 1) {
        const unsigned lgo = WS_LOGIT + ((b & ~1) | (cw >> 1)) * 256u + (32 * (cw & 1) + (late(lane) & 15)) * 4u;
#pragma unroll
        for (int a = 0; a < 2; ++a) lg[a] = __builtin_bit_cast(float, __builtin_amdgcn_raw_buffer_load_b32(rWs, lgo + 64 * a, 0, AUX_SC1));
      }
      static_for<0, 14>([&](auto U) {
        constexpr int u = U.value, kap = u / 7, m = u % 7, bi = (14 * (n - N2) + u) % 3;
        if constexpr (u == 12) {
          WAITLGKM0();
          if constexpr (n + 1 == B_FLAG1) WAITVM(0);
          BAR();
          if constexpr (n + 1 == B_FLAG1) { if (cw == 0 && lane == 0) st_flag(ws, myflag1, tag); }
        }
        constexpr bool nxt = n + 1 < N3;
        if constexpr (u + 2 < 14) rdW(N, IC<u + 2>{});
        else if constexpr (nxt) rdW(IC<n + 1>{}, IC<u + 2 - 14>{});
        if constexpr (u == 2) rdT(N, IC<1>{});
        if constexpr (u == 12 && nxt) rdT(IC<n + 1>{}, IC<0>{});
#pragma unroll
        for (int a = 0; a < 2; ++a) {
          hg[a][m] = mfma(wg[bi], t[kap][a], hg[a][m]);
          hu[a][m] = mfma(wu[bi], t[kap][a], hu[a][m]);
        }
        constexpr int nr = ((u + 2 < 14 || nxt) ? 4 : 0) + ((u == 2 || (u == 12 && nxt)) ? 2 : 0);
        if constexpr (nr > 0) interleave<nr, 4>();
        SBAR();
      });
    });
  }
  float gs[2];
  {
    const unsigned hb = WS_H + (I * 4 + q) * 114688u + (2 * cw) * 1024u + late(lane) * 16u;
    const unsigned hp = hb + ((7 * j + 1) >> 1) * 8192u;
    const unsigned hs = hb + ((j & 1) ? ((7 * j) >> 1) * 8192u + 8u : (((7 * j) >> 1) + 3) * 8192u);
#pragma unroll
    for (int a = 0; a < 2; ++a) {
      const float L2E = 1.44269504088896341f;
      gs[a] = __builtin_amdgcn_rcpf(1.f + __builtin_amdgcn_exp2f(-L2E * lg[a]));
      const float inv = fminf(1.f + __builtin_amdgcn_exp2f(L2E * lg[a]), 1e30f);
      unsigned h2[7][2];
#pragma unroll
      for (int m = 0; m < 7; ++m) {
        float h[4];
#pragma unroll
        for (int r = 0; r < 4; ++r) {
          const float e = __builtin_amdgcn_exp2f(-L2E * hg[a][m][r]);
          h[r] = hu[a][m][r] * __builtin_amdgcn_rcpf(__builtin_fmaf(e, inv, inv));
        }
        h2[m][0] = pk2(h[0], h[1]);
        h2[m][1] = pk2(h[2], h[3]);
      }
#pragma unroll
      for (int v = 0; v < 3; ++v) {
        const u32x4 d = {h2[2 * v][0], h2[2 * v][1], h2[2 * v + 1][0], h2[2 * v + 1][1]};
        __builtin_amdgcn_raw_buffer_store_b128(d, rWs, hp + v * 8192 + a * 1024, 0, AUX_SC1);
      }
      const u32x2 d2 = {h2[6][0], h2[6][1]};
      __builtin_amdgcn_raw_buffer_store_b64(d2, rWs, hs + a * 1024, 0, AUX_SC1);
    }
  }

  f32x4 acc[2][7];
#pragma unroll
  for (int a = 0; a < 2; ++a)
#pragma unroll
    for (int m = 0; m < 7; ++m) acc[a][m] = f32x4{0, 0, 0, 0};
  {
    const int l3 = late(lane), g = l3 >> 4, c = l3 & 15;
    const int oT0 = (32 * cw + c) * 128 + ((g ^ (c >> 1)) << 4), oT1 = oT0 ^ 64;
    const int oE = 16384 + l3 * 16;
    const int oH = 2 * cw * 1024 + l3 * 16;
    const int oD = 16384 + (4 * g + (c >> 2)) * 224 + 8 * (c & 3);
    const unsigned myflag2 = WS_FLAG2 + ((I * 4 + q) * 4 + j) * 8u;
    bf16x8 ft[2][2], fe[2][7];
    auto rd = [&](auto N, auto H) {
      constexpr int n = N.value, h = H.value;
      LAS char* slot = lds + st_off<n>();
      if constexpr (n < N4) {
#pragma unroll
        for (int a = 0; a < 2; ++a) ft[h][a] = ldsr(slot + (h ? oT1 : oT0) + a * 2048);
#pragma unroll
        for (int m = 0; m < 7; ++m) fe[h][m] = ldsr(slot + oE + h * 7168 + m * 1024);
      } else {
#pragma unroll
        for (int a = 0; a < 2; ++a) ft[h][a] = ldsr(slot + oH + h * 8192 + a * 1024);
#pragma unroll
        for (int m = 0; m < 7; ++m) {
          LAS char* p = slot + oD + h * 7168 + m * 32;
          fe[h][m] = ldstr(p, p + 3584);
        }
      }
    };
    auto mm = [&](auto H) {
      constexpr int h = H.value;
#pragma unroll
      for (int m = 0; m < 7; ++m)
#pragma unroll
        for (int a = 0; a < 2; ++a) acc[a][m] = mfma(fe[h][m], ft[h][a], acc[a][m]);
    };
    rd(IC<N3>{}, IC<0>{});
    static_for<N3, NST>([&](auto N) {
      constexpr int n = N.value;
      rd(N, IC<1>{});
      mm(IC<0>{});
      interleave<(n < N4 ? 9 : 16), 14>();
      SBAR();
      if constexpr (n + 1 < NST) {
        WAITLGKM0();
        if constexpr (n + 1 == B_FLAG2) WAITVM(0);
        BAR();
        if constexpr (n + 1 == B_FLAG2) { if (cw == 0 && lane == 0) st_flag(ws, myflag2, tag); }
        rd(IC<n + 1>{}, IC<0>{});
      }
      mm(IC<1>{});
      if constexpr (n + 1 < NST) interleave<(n + 1 < N4 ? 9 : 16), 14>();
      SBAR();
      if constexpr (n + 1 == N4) {
#pragma unroll
        for (int a = 0; a < 2; ++a)
#pragma unroll
          for (int m = 0; m < 7; ++m) acc[a][m] *= gs[a];
        SBAR();
      }
    });
  }

  const int l4 = late(lane), g = l4 >> 4, c = l4 & 15;
  const unsigned inb = WS_INBOX + b * 32768u + cw * 2048u + l4 * 16u;
  f32x4 own[4];
#pragma unroll
  for (int k = 0; k < 4; ++k) own[k] = f32x4{0, 0, 0, 0};
  static_for<0, 4>([&](auto Q) {
    constexpr int qd = Q.value, a = qd & 1, mb = (qd >> 1) * 4;
    if (qd != q) {
      unsigned d[8];
#pragma unroll
      for (int k = 0; k < 4; ++k) {
        if (mb + k < 7) {
          const f32x4 x = acc[a][mb + k < 7 ? mb + k : 0];
          const unsigned u0 = pk2(x[0], x[1]), u1 = pk2(x[2], x[3]);
          d[2 * k] = u0 < SENT ? u0 : SENT - 1;
          d[2 * k + 1] = u1 < SENT ? u1 : SENT - 1;
        } else { d[2 * k] = 0; d[2 * k + 1] = 0; }
      }
      const unsigned o = WS_INBOX + ((b & ~3) | qd) * 32768u + q * 8192u + cw * 2048u + l4 * 16u;
      __builtin_amdgcn_raw_buffer_store_b128(u32x4{d[0], d[1], d[2], d[3]}, rWs, o, 0, AUX_SC1);
      __builtin_amdgcn_raw_buffer_store_b128(u32x4{d[4], d[5], d[6], d[7]}, rWs, o + 1024, 0, AUX_SC1);
    } else {
#pragma unroll
      for (int k = 0; k < 4; ++k) if (mb + k < 7) own[k] = acc[a][mb + k < 7 ? mb + k : 0];
    }
  });
  u32x4 in[6], sa[6], sb[6];
  const int mb = (q >> 1) * 4;
  unsigned ob = (128 * I + 32 * cw + 16 * (q & 1) + c) * 896u + (112 * j + 16 * mb + 4 * g) * 2u;
  {
    unsigned o0 = inb + ((q + 1) & 3) * 8192u, o1 = inb + ((q + 2) & 3) * 8192u, o2 = inb + ((q + 3) & 3) * 8192u;
    unsigned left, have;
    asm volatile("" : "+v"(ob));
    asm volatile(
      "s_nop 4\n\t"
      "s_mov_b32 %[left], 0x100000\n\t"
      "s_mov_b32 %[have], 0\n\t"
      "buffer_load_dwordx4 v[128:131], %[o0], %[rs], 0 offen sc1\n\t"
      "buffer_load_dwordx4 v[132:135], %[o0], %[rs], 0 offen offset:1024 sc1\n\t"
      "buffer_load_dwordx4 v[136:139], %[o1], %[rs], 0 offen sc1\n\t"
      "buffer_load_dwordx4 v[140:143], %[o1], %[rs], 0 offen offset:1024 sc1\n\t"
      "buffer_load_dwordx4 v[144:147], %[o2], %[rs], 0 offen sc1\n\t"
      "buffer_load_dwordx4 v[148:151], %[o2], %[rs], 0 offen offset:1024 sc1\n\t"
      "s_sleep 12\n\t"
      ".Lfk_poll:\n\t"
      "buffer_load_dwordx4 v[152:155], %[o0], %[rs], 0 offen sc1\n\t"
      "buffer_load_dwordx4 v[156:159], %[o0], %[rs], 0 offen offset:1024 sc1\n\t"
      "buffer_load_dwordx4 v[160:163], %[o1], %[rs], 0 offen sc1\n\t"
      "buffer_load_dwordx4 v[164:167], %[o1], %[rs], 0 offen offset:1024 sc1\n\t"
      "buffer_load_dwordx4 v[168:171], %[o2], %[rs], 0 offen sc1\n\t"
      "buffer_load_dwordx4 v[172:175], %[o2], %[rs], 0 offen offset:1024 sc1\n\t"
      "s_waitcnt vmcnt(6)\n\t"
      "s_bitcmp1_b32 %[have], 0\n\t"
      "s_cbranch_scc1 .Lfk_skip_0_0\n\t"
      "v_max3_u32 v200, v128, v129, v130\n\t"
      "v_max3_u32 v200, v200, v131, v132\n\t"
      "v_max3_u32 v200, v200, v133, v134\n\t"
      "v_max_u32 v200, v200, v135\n\t"
      "v_cmp_eq_u32 vcc, -1, v200\n\t"
      "s_cmp_eq_u64 vcc, 0\n\t"
      "s_cbranch_scc0 .Lfk_skip_0_0\n\t"
      "v_mov_b64 v[176:177], v[128:129]\n\t"
      "v_mov_b64 v[178:179], v[130:131]\n\t"
      "v_mov_b64 v[180:181], v[132:133]\n\t"
      "v_mov_b64 v[182:183], v[134:135]\n\t"
      "v_mov_b32 %[o0], %[oor]\n\t"
      "s_bitset1_b32 %[have], 0\n\t"
      ".Lfk_skip_0_0:\n\t"
      "s_bitcmp1_b32 %[have], 1\n\t"
      "s_cbranch_scc1 .Lfk_skip_0_1\n\t"
      "v_max3_u32 v200, v136, v137, v138\n\t"
      "v_max3_u32 v200, v200, v139, v140\n\t"
      "v_max3_u32 v200, v200, v141, v142\n\t"
      "v_max_u32 v200, v200, v143\n\t"
      "v_cmp_eq_u32 vcc, -1, v200\n\t"
      "s_cmp_eq_u64 vcc, 0\n\t"
      "s_cbranch_scc0 .Lfk_skip_0_1\n\t"
      "v_mov_b64 v[184:185], v[136:137]\n\t"
      "v_mov_b64 v[186:187], v[138:139]\n\t"
      "v_mov_b64 v[188:189], v[140:141]\n\t"
      "v_mov_b64 v[190:191], v[142:143]\n\t"
      "v_mov_b32 %[o1], %[oor]\n\t"
      "s_bitset1_b32 %[have], 1\n\t"
      ".Lfk_skip_0_1:\n\t"
      "s_bitcmp1_b32 %[have], 2\n\t"
      "s_cbranch_scc1 .Lfk_skip_0_2\n\t"
      "v_max3_u32 v200, v144, v145, v146\n\t"
      "v_max3_u32 v200, v200, v147, v148\n\t"
      "v_max3_u32 v200, v200, v149, v150\n\t"
      "v_max_u32 v200, v200, v151\n\t"
      "v_cmp_eq_u32 vcc, -1, v200\n\t"
      "s_cmp_eq_u64 vcc, 0\n\t"
      "s_cbranch_scc0 .Lfk_skip_0_2\n\t"
      "v_mov_b64 v[192:193], v[144:145]\n\t"
      "v_mov_b64 v[194:195], v[146:147]\n\t"
      "v_mov_b64 v[196:197], v[148:149]\n\t"
      "v_mov_b64 v[198:199], v[150:151]\n\t"
      "v_mov_b32 %[o2], %[oor]\n\t"
      "s_bitset1_b32 %[have], 2\n\t"
      ".Lfk_skip_0_2:\n\t"
      "s_cmp_eq_u32 %[have], 7\n\t"
      "s_cbranch_scc1 .Lfk_poll_end\n\t"
      "buffer_load_dwordx4 v[128:131], %[o0], %[rs], 0 offen sc1\n\t"
      "buffer_load_dwordx4 v[132:135], %[o0], %[rs], 0 offen offset:1024 sc1\n\t"
      "buffer_load_dwordx4 v[136:139], %[o1], %[rs], 0 offen sc1\n\t"
      "buffer_load_dwordx4 v[140:143], %[o1], %[rs], 0 offen offset:1024 sc1\n\t"
      "buffer_load_dwordx4 v[144:147], %[o2], %[rs], 0 offen sc1\n\t"
      "buffer_load_dwordx4 v[148:151], %[o2], %[rs], 0 offen offset:1024 sc1\n\t"
      "s_waitcnt vmcnt(6)\n\t"
      "s_bitcmp1_b32 %[have], 0\n\t"
      "s_cbranch_scc1 .Lfk_skip_1_0\n\t"
      "v_max3_u32 v200, v152, v153, v154\n\t"
      "v_max3_u32 v200, v200, v155, v156\n\t"
      "v_max3_u32 v200, v200, v157, v158\n\t"
      "v_max_u32 v200, v200, v159\n\t"
      "v_cmp_eq_u32 vcc, -1, v200\n\t"
      "s_cmp_eq_u64 vcc, 0\n\t"
      "s_cbranch_scc0 .Lfk_skip_1_0\n\t"
      "v_mov_b64 v[176:177], v[152:153]\n\t"
      "v_mov_b64 v[178:179], v[154:155]\n\t"
      "v_mov_b64 v[180:181], v[156:157]\n\t"
      "v_mov_b64 v[182:183], v[158:159]\n\t"
      "v_mov_b32 %[o0], %[oor]\n\t"
      "s_bitset1_b32 %[have], 0\n\t"
      ".Lfk_skip_1_0:\n\t"
      "s_bitcmp1_b32 %[have], 1\n\t"
      "s_cbranch_scc1 .Lfk_skip_1_1\n\t"
      "v_max3_u32 v200, v160, v161, v162\n\t"
      "v_max3_u32 v200, v200, v163, v164\n\t"
      "v_max3_u32 v200, v200, v165, v166\n\t"
      "v_max_u32 v200, v200, v167\n\t"
      "v_cmp_eq_u32 vcc, -1, v200\n\t"
      "s_cmp_eq_u64 vcc, 0\n\t"
      "s_cbranch_scc0 .Lfk_skip_1_1\n\t"
      "v_mov_b64 v[184:185], v[160:161]\n\t"
      "v_mov_b64 v[186:187], v[162:163]\n\t"
      "v_mov_b64 v[188:189], v[164:165]\n\t"
      "v_mov_b64 v[190:191], v[166:167]\n\t"
      "v_mov_b32 %[o1], %[oor]\n\t"
      "s_bitset1_b32 %[have], 1\n\t"
      ".Lfk_skip_1_1:\n\t"
      "s_bitcmp1_b32 %[have], 2\n\t"
      "s_cbranch_scc1 .Lfk_skip_1_2\n\t"
      "v_max3_u32 v200, v168, v169, v170\n\t"
      "v_max3_u32 v200, v200, v171, v172\n\t"
      "v_max3_u32 v200, v200, v173, v174\n\t"
      "v_max_u32 v200, v200, v175\n\t"
      "v_cmp_eq_u32 vcc, -1, v200\n\t"
      "s_cmp_eq_u64 vcc, 0\n\t"
      "s_cbranch_scc0 .Lfk_skip_1_2\n\t"
      "v_mov_b64 v[192:193], v[168:169]\n\t"
      "v_mov_b64 v[194:195], v[170:171]\n\t"
      "v_mov_b64 v[196:197], v[172:173]\n\t"
      "v_mov_b64 v[198:199], v[174:175]\n\t"
      "v_mov_b32 %[o2], %[oor]\n\t"
      "s_bitset1_b32 %[have], 2\n\t"
      ".Lfk_skip_1_2:\n\t"
      "s_cmp_eq_u32 %[have], 7\n\t"
      "s_cbranch_scc1 .Lfk_poll_end\n\t"
      "s_sub_u32 %[left], %[left], 1\n\t"
      "s_cbranch_scc0 .Lfk_poll\n"
      ".Lfk_poll_end:"
      : "=&{v[176:179]}"(in[0]), "=&{v[180:183]}"(in[1]), "=&{v[184:187]}"(in[2]), "=&{v[188:191]}"(in[3]), "=&{v[192:195]}"(in[4]), "=&{v[196:199]}"(in[5]),
        "=&{v[128:131]}"(sa[0]), "=&{v[132:135]}"(sa[1]), "=&{v[136:139]}"(sa[2]), "=&{v[140:143]}"(sa[3]), "=&{v[144:147]}"(sa[4]), "=&{v[148:151]}"(sa[5]),
        "=&{v[152:155]}"(sb[0]), "=&{v[156:159]}"(sb[1]), "=&{v[160:163]}"(sb[2]), "=&{v[164:167]}"(sb[3]), "=&{v[168:171]}"(sb[4]), "=&{v[172:175]}"(sb[5]),
        [left] "=&s"(left), [have] "=&s"(have), [o0] "+v"(o0), [o1] "+v"(o1), [o2] "+v"(o2)
      : [rs] "s"(rWs), [oor] "s"(0x80000000u)
      : "v200", "vcc", "scc", "memory");
  }
  {
    const rsrc_t rO = mkrs(out, 2048u * 896u);
#pragma unroll
    for (int k = 0; k < 4; ++k) {
      f32x4 s = own[k];
#pragma unroll
      for (int d = 0; d < 3; ++d) {
        const unsigned u0 = in[2 * d + (k >> 1)][2 * (k & 1)], u1 = in[2 * d + (k >> 1)][2 * (k & 1) + 1];
        s += f32x4{bflo(u0), bfhi(u0), bflo(u1), bfhi(u1)};
      }
      if (k < 3 || mb == 0) {
        const u32x2 o2 = {pk2(s[0], s[1]), pk2(s[2], s[3])};
        __builtin_amdgcn_raw_buffer_store_b64(o2, rO, ob + 32 * k, 0, 0);
      }
    }
  }
  asm volatile("s_waitcnt vmcnt(3)"
      :: "{v[128:131]}"(sa[0]), "{v[132:135]}"(sa[1]), "{v[136:139]}"(sa[2]), "{v[140:143]}"(sa[3]), "{v[144:147]}"(sa[4]), "{v[148:151]}"(sa[5]),
         "{v[152:155]}"(sb[0]), "{v[156:159]}"(sb[1]), "{v[160:163]}"(sb[2]), "{v[164:167]}"(sb[3]), "{v[168:171]}"(sb[4]), "{v[172:175]}"(sb[5])
      : "memory");
}

__global__ __launch_bounds__(512, 2) void fused_kernel(const void* Ax, const void* Cre, const void* Cim, const void* Wdre, const void* Wdim,
                                                       const void* Wg, const void* Wu, const void* Wdn, const void* gw, void* out, char* ws) {
  __shared__ __attribute__((aligned(1024))) char shm[LDS_BYTES];
  LAS char* lds = (LAS char*)shm;
  {
    SBAR();
    u64 pc;
    unsigned len, o0, o1, oc;
    asm volatile("s_getpc_b64 %0\n.Lfk_entry:\n\ts_mov_b32 %1, .Lfk_code_end-.Lfk_entry\n\ts_mov_b32 %2, .Lfk_loader0-.Lfk_entry-256\n\t"
                 "s_mov_b32 %3, .Lfk_loader1-.Lfk_entry-256\n\ts_mov_b32 %4, .Lfk_loaders-.Lfk_entry"
                 : "=s"(pc), "=s"(len), "=s"(o0), "=s"(o1), "=s"(oc));
    const unsigned w = threadIdx.x >> 6, l = threadIdx.x & 63;
    const unsigned o = w < 2 && l < 36 ? (l < 4 ? oc : w ? o1 : o0) + l * 64u : 0x80000000u;
    __builtin_amdgcn_raw_ptr_buffer_load_lds(mkrs((const void*)pc, len), (LAS void*)(lds + PF_OFF), 4, o, 0, 0, 0);
    SBAR();
  }
  asm volatile("" : "+s"(Ax), "+s"(Cre), "+s"(Cim), "+s"(Wdre), "+s"(Wdim), "+s"(Wg), "+s"(Wu), "+s"(Wdn), "+s"(gw), "+s"(out), "+s"(ws));
  Geo G;
  G.lane = threadIdx.x & 63;
  G.wid = __builtin_amdgcn_readfirstlane(threadIdx.x >> 6);
  G.b = blockIdx.x;
  G.q = G.b & 3;
  G.r = (G.b >> 2) & 1;
  G.j = (G.b >> 3) & 3;
  G.i = G.b >> 5;
  G.I = 8 * G.r + G.i;
  G.k7 = (G.i + 2 * G.j) % 7;
  G.k16 = 2 * G.i;
  if (G.wid >= 4) {
    asm volatile(".Lfk_loaders:");
    SBAR();
    const bool im = G.q >= 2;
    const void* Cp = im ? Cim : Cre;
    const void* Wdp = im ? Wdim : Wdre;
    if (G.wid < 6) loader<0>(G, lds, ws, Ax, Cp, Wdp, Wg, Wu, Wdn);
    else loader<1>(G, lds, ws, Ax, Cp, Wdp, Wg, Wu, Wdn);
  } else {
    compute(G, lds, ws, gw, out, Wg, Wu, Wdn, G.q >= 2 ? Cim : Cre);
  }
  asm volatile(".Lfk_code_end:");
}

}

extern "C" void kernel_launch(void* const* d_in, const int* in_sizes, int n_in, void* d_out, int out_size, void* d_ws,
                              size_t ws_size, hipStream_t stream) {
  (void)in_sizes; (void)n_in; (void)out_size; (void)ws_size;
  fk::fused_kernel<<<256, 512, 0, stream>>>(d_in[0], d_in[1], d_in[2], d_in[3], d_in[4], d_in[6], d_in[7], d_in[8], d_in[9],
                                            d_out, (char*)d_ws);
}
```
